```python
import torch

def _softmax(x, axis=-1):
    return torch.softmax(x, dim=axis)
def _gelu(x):
    return torch.nn.functional.gelu(x, approximate="tanh")
def _silu(x):
    return torch.nn.functional.silu(x)
def _sigmoid(x):
    return torch.sigmoid(x)
def _layernorm(x, axis=-1, eps=1e-5):
    m = x.mean(dim=axis, keepdim=True)
    v = x.var(dim=axis, keepdim=True, unbiased=False)
    return (x - m) / torch.sqrt(v + eps)
def _scatter_add(tgt, idx, src, axis=0):
    return tgt.clone().index_add_(axis, idx, src)
def _segment_sum(src, seg, n):
    out = torch.zeros((n,) + src.shape[1:], dtype=src.dtype)
    return out.index_add_(0, seg, src)
def _pad(x, pw):
    flat = tuple(v for pair in reversed(pw) for v in pair)
    return torch.nn.functional.pad(x, flat)

kernel_name = 'gated_mix__sliding_block__geglu_ffn__prompt'

def setup_inputs():

    _w = (lambda a: (lambda t: t.to(torch.bfloat16) if t.is_floating_point() else t)(torch.as_tensor(a)))
    def _A_setup():
        B, S, D = 1, 2048, 1024
        return {
            "x": _w((lambda *s: torch.randn(*s, dtype=torch.bfloat16) * 0.05)(B, S, D)),
            "wqkv": _w((lambda *s: torch.randn(*s, dtype=torch.bfloat16) * 1.6)(D, 3 * D)),
            "wo": _w((lambda *s: torch.randn(*s, dtype=torch.bfloat16) * 0.1)(D, D)),
        }
    _A_in = _A_setup()

    def _B_setup():
        B, S, D, F = 1, 2048, 1024, 3584
        return {
            "x": _w((lambda *s: torch.randn(*s, dtype=torch.bfloat16) * 0.05)(B, S, D)),
            "w_gate": _w((lambda *s: torch.randn(*s, dtype=torch.bfloat16) * 0.4)(D, F)),
            "w_up": _w((lambda *s: torch.randn(*s, dtype=torch.bfloat16) * 0.4)(D, F)),
            "w_down": _w((lambda *s: torch.randn(*s, dtype=torch.bfloat16) * 0.4)(F, D)),
        }
    _B_in = _B_setup()

    return {
        "A_x": _A_in["x"],
        "A_wqkv": _A_in["wqkv"],
        "A_wo": _A_in["wo"],
        "B_x": _B_in["x"],
        "B_w_gate": _B_in["w_gate"],
        "B_w_up": _B_in["w_up"],
        "B_w_down": _B_in["w_down"],
        "gate_w": _w((lambda *s: torch.randn(*s, dtype=torch.bfloat16) * 0.05)(1024) * 16.0),
    }

def reference(A_x, A_wqkv, A_wo, B_x, B_w_gate, B_w_up, B_w_down, gate_w):
    def _A_ref(x, wqkv, wo):
        B, S, D = x.shape
        H = 32; Dh = D // H; W = 256
        qkv = (lambda x, *d: x.permute(*d))((x @ wqkv).reshape(B, S, 3, H, Dh), 2, 0, 3, 1, 4)
        q, k, v = qkv[0], qkv[1], qkv[2]
        scores = torch.einsum("bhqd,bhkd->bhqk", q, k) * (Dh ** -0.5)
        idx = torch.arange(S)
        mask = (idx[:, None] - idx[None, :] >= 0) & (idx[:, None] - idx[None, :] < W)
        scores = torch.where(mask[None, None], scores, -1e9)
        p = _softmax(scores, axis=-1)
        o = torch.einsum("bhqk,bhkd->bhqd", p, v)
        return (lambda x, *d: x.permute(*d))(o, 0, 2, 1, 3).reshape(B, S, D) @ wo

    def _B_ref(x, w_gate, w_up, w_down):
        return (_gelu(x @ w_gate) * (x @ w_up)) @ w_down

    x = A_x
    g = _sigmoid((x * gate_w).sum(-1, keepdims=True))
    a = _A_ref(x, A_wqkv, A_wo)
    b = _B_ref(x, B_w_gate, B_w_up, B_w_down)
    return g * a + (1 - g) * b
```

```cpp
#include <hip/hip_runtime.h>
#include <hip/hip_bf16.h>
typedef unsigned short u16;
__device__ __forceinline__ float bf2f(u16 v) { return __uint_as_float(((unsigned)v) << 16); }
__device__ __forceinline__ u16 f2bf(float f) { __hip_bfloat16 b = __float2bfloat16(f); return *(u16*)&b; }

__global__ void gemm_naive(const u16* A, const u16* B, float* C, int M, int N, int K) {
  __shared__ float As[16][17], Bs[16][17];
  int tx = threadIdx.x, ty = threadIdx.y;
  int row = blockIdx.y * 16 + ty, col = blockIdx.x * 16 + tx;
  float acc = 0.f;
  for (int k0 = 0; k0 < K; k0 += 16) {
    As[ty][tx] = bf2f(A[(size_t)row * K + k0 + tx]);
    Bs[ty][tx] = bf2f(B[(size_t)(k0 + ty) * N + col]);
    __syncthreads();
    for (int k = 0; k < 16; ++k) acc += As[ty][k] * Bs[k][tx];
    __syncthreads();
  }
  C[(size_t)row * N + col] = acc;
}
__global__ void to_bf16(const float* in, u16* out, int n) { int i = blockIdx.x * 256 + threadIdx.x; if (i < n) out[i] = f2bf(in[i]); }
__global__ void glu(const float* g, const float* u, u16* h, int n) {
  int i = blockIdx.x * 256 + threadIdx.x; if (i >= n) return;
  float x = g[i]; float t = tanhf(0.7978845608f * (x + 0.044715f * x * x * x));
  h[i] = f2bf(0.5f * x * (1.f + t) * u[i]);
}
__global__ void attn_naive(const u16* qkv, u16* o) {
  int q = blockIdx.x * 64 + threadIdx.x; int h = blockIdx.y;
  float qv[32]; for (int d = 0; d < 32; ++d) qv[d] = bf2f(qkv[(size_t)q * 3072 + h * 32 + d]);
  int k0 = q - 255; if (k0 < 0) k0 = 0;
  float m = -1e30f;
  for (int k = k0; k <= q; ++k) { float s = 0; for (int d = 0; d < 32; ++d) s += qv[d] * bf2f(qkv[(size_t)k * 3072 + 1024 + h * 32 + d]); s *= 0.17677669529f; m = fmaxf(m, s); }
  float l = 0; float acc[32]; for (int d = 0; d < 32; ++d) acc[d] = 0;
  for (int k = k0; k <= q; ++k) { float s = 0; for (int d = 0; d < 32; ++d) s += qv[d] * bf2f(qkv[(size_t)k * 3072 + 1024 + h * 32 + d]); s *= 0.17677669529f; float p = expf(s - m); l += p;
    for (int d = 0; d < 32; ++d) acc[d] += p * bf2f(qkv[(size_t)k * 3072 + 2048 + h * 32 + d]); }
  for (int d = 0; d < 32; ++d) o[(size_t)q * 1024 + h * 32 + d] = f2bf(acc[d] / l);
}
__global__ void final_mix(const u16* x, const u16* gw, const float* a, const float* b, u16* out) {
  int row = blockIdx.x; __shared__ float red[256];
  float s = 0; for (int i = threadIdx.x; i < 1024; i += 256) s += bf2f(x[row * 1024 + i]) * bf2f(gw[i]);
  red[threadIdx.x] = s; __syncthreads();
  for (int o = 128; o > 0; o >>= 1) { if (threadIdx.x < o) red[threadIdx.x] += red[threadIdx.x + o]; __syncthreads(); }
  float g = 1.f / (1.f + expf(-red[0]));
  for (int i = threadIdx.x; i < 1024; i += 256) out[row * 1024 + i] = f2bf(g * a[row * 1024 + i] + (1.f - g) * b[row * 1024 + i]);
}
extern "C" void kernel_launch(void* const* d_in, const int* in_sizes, int n_in, void* d_out, int out_size, void* d_ws, size_t ws_size, hipStream_t stream) {
  const u16* x = (const u16*)d_in[0]; const u16* wqkv = (const u16*)d_in[1]; const u16* wo = (const u16*)d_in[2];
  const u16* wg = (const u16*)d_in[4]; const u16* wu = (const u16*)d_in[5]; const u16* wd = (const u16*)d_in[6]; const u16* gw = (const u16*)d_in[7];
  char* ws = (char*)d_ws;
  float* f0 = (float*)ws;
  float* f1 = (float*)(ws + (32u << 20));
  u16* qkv = (u16*)(ws + (64u << 20));
  u16* o = (u16*)(ws + (80u << 20));
  u16* h = (u16*)(ws + (96u << 20));
  float* a = (float*)(ws + (112u << 20));
  float* b = (float*)(ws + (128u << 20));
  dim3 t(16, 16);
  gemm_naive<<<dim3(3072 / 16, 128), t, 0, stream>>>(x, wqkv, f0, 2048, 3072, 1024);
  to_bf16<<<2048 * 3072 / 256, 256, 0, stream>>>(f0, qkv, 2048 * 3072);
  attn_naive<<<dim3(32, 32), 64, 0, stream>>>(qkv, o);
  gemm_naive<<<dim3(1024 / 16, 128), t, 0, stream>>>(o, wo, a, 2048, 1024, 1024);
  gemm_naive<<<dim3(3584 / 16, 128), t, 0, stream>>>(x, wg, f0, 2048, 3584, 1024);
  gemm_naive<<<dim3(3584 / 16, 128), t, 0, stream>>>(x, wu, f1, 2048, 3584, 1024);
  glu<<<2048 * 3584 / 256, 256, 0, stream>>>(f0, f1, h, 2048 * 3584);
  gemm_naive<<<dim3(1024 / 16, 128), t, 0, stream>>>(h, wd, b, 2048, 1024, 3584);
  final_mix<<<2048, 256, 0, stream>>>(x, gw, a, b, (u16*)d_out);
}
```

```cpp
#include <hip/hip_runtime.h>
#include <hip/hip_bf16.h>

#define LAS __attribute__((address_space(3)))
typedef unsigned short u16;
typedef short s16x4 __attribute__((ext_vector_type(4)));
typedef short s16x8 __attribute__((ext_vector_type(8)));
typedef __bf16 bf16x8 __attribute__((ext_vector_type(8)));
typedef __bf16 bf16x2 __attribute__((ext_vector_type(2)));
typedef float f32x2 __attribute__((ext_vector_type(2)));
typedef float f32x4 __attribute__((ext_vector_type(4)));
typedef float f32x16 __attribute__((ext_vector_type(16)));
typedef unsigned u32x2 __attribute__((ext_vector_type(2)));
typedef unsigned u32x4 __attribute__((ext_vector_type(4)));
typedef __amdgpu_buffer_rsrc_t rsrc_t;

constexpr int S_ = 2048, D_ = 1024, F_ = 3584;
constexpr size_t WS_A2 = 0;
constexpr size_t WS_Q  = 20u << 20;
constexpr size_t WS_K  = 24u << 20;
constexpr size_t WS_VT = 28u << 20;
constexpr size_t WS_G  = 32u << 20;
constexpr size_t WS_FLAG = 33u << 20;
constexpr size_t WS_PART = 40u << 20;
constexpr unsigned A2_BYTES = 288u * 2048u * 32u;

#define STAMP_DECL
#define STAMP_AT(i)
#define STAMP_FLUSH(base, n)
#define BAR() do { asm volatile("" ::: "memory"); __builtin_amdgcn_s_barrier(); asm volatile("" ::: "memory"); } while (0)
#define SCHED() __builtin_amdgcn_sched_barrier(0)
#define WAIT_VL(n) do { asm volatile("s_waitcnt vmcnt(" #n ")" ::: "memory"); __builtin_amdgcn_s_waitcnt(0xC07F); } while (0)
#define WAIT_V(n) asm volatile("s_waitcnt vmcnt(" #n ")" ::: "memory")
#define WAIT_L0() do { asm volatile("" ::: "memory"); __builtin_amdgcn_s_waitcnt(0xC07F); } while (0)

__device__ __forceinline__ rsrc_t mk_rsrc(const void* p, unsigned bytes) {
  return __builtin_amdgcn_make_buffer_rsrc((void*)p, (short)0, (int)bytes, 0x00020000);
}
template <int MC> __device__ __forceinline__ void dma64(unsigned mbase, unsigned voff, rsrc_t srd, unsigned soff) {
  asm volatile("s_add_u32 m0, %0, %4\n\ts_nop 0\n\tbuffer_load_dwordx4 %1, %2, %3 offen lds"
               :: "s"(mbase), "v"(voff), "s"(srd), "s"(soff), "i"(MC) : "memory", "scc");
}
template <int MC> __device__ __forceinline__ void dma64_o128(unsigned mbase, unsigned voff, rsrc_t srd, unsigned soff) {
  asm volatile("s_add_u32 m0, %0, %4\n\ts_nop 0\n\tbuffer_load_dwordx4 %1, %2, %3 offen offset:128 lds"
               :: "s"(mbase), "v"(voff), "s"(srd), "s"(soff), "i"(MC - 128) : "memory", "scc");
}
template <int MC> __device__ __forceinline__ void dma48_o128(unsigned mbase, unsigned voff, rsrc_t srd, unsigned soff) {
  asm volatile("s_add_u32 m0, %0, %4\n\ts_mov_b32 exec_hi, 0xffff\n\tbuffer_load_dwordx4 %1, %2, %3 offen offset:128 lds\n\ts_mov_b32 exec_hi, -1"
               :: "s"(mbase), "v"(voff), "s"(srd), "s"(soff), "i"(MC - 128) : "memory", "scc");
}
template <int MC> __device__ __forceinline__ void dma32(unsigned mbase, unsigned voff, rsrc_t srd, unsigned soff) {
  asm volatile("s_add_u32 m0, %0, %4\n\ts_mov_b32 exec_hi, 0\n\tbuffer_load_dwordx4 %1, %2, %3 offen lds\n\ts_mov_b32 exec_hi, -1"
               :: "s"(mbase), "v"(voff), "s"(srd), "s"(soff), "i"(MC) : "memory", "scc");
}
__device__ __forceinline__ bf16x8 lds_b128(LAS char* p) { return *(LAS bf16x8*)p; }
__device__ __forceinline__ bf16x8 lds_tr2(LAS char* p) {
  s16x4 a = __builtin_amdgcn_ds_read_tr16_b64_v4i16((LAS s16x4*)p);
  s16x4 b = __builtin_amdgcn_ds_read_tr16_b64_v4i16((LAS s16x4*)(p + 128));
  s16x8 r = __builtin_shufflevector(a, b, 0, 1, 2, 3, 4, 5, 6, 7);
  return __builtin_bit_cast(bf16x8, r);
}
__device__ __forceinline__ unsigned pk_bf16(float lo, float hi) {
  bf16x2 v; v.x = (__bf16)lo; v.y = (__bf16)hi; return __builtin_bit_cast(unsigned, v);
}
__device__ __forceinline__ float bf_lo(unsigned u) { return __uint_as_float(u << 16); }
__device__ __forceinline__ float bf_hi(unsigned u) { return __uint_as_float(u & 0xffff0000u); }
#define MFMA16(a, b, c) __builtin_amdgcn_mfma_f32_16x16x32_bf16((a), (b), (c), 0, 0, 0)
#define MFMA32(a, b, c) __builtin_amdgcn_mfma_f32_32x32x16_bf16((a), (b), (c), 0, 0, 0)

__device__ __forceinline__ float geglu(float g, float u) {
  const float c1 = -2.0f * 0.7978845608028654f * 1.4426950408889634f;
  const float c2 = c1 * 0.044715f;
  float z = g * (c1 + c2 * g * g);
  float e = __builtin_amdgcn_exp2f(z);
  return g * u * __builtin_amdgcn_rcpf(1.0f + e);
}

namespace k1 {
constexpr int XP = 1088, XH = 16 * XP;
constexpr int X0 = 0;
constexpr int GU_RB = 14 * 256 + 128, GU_ST = 8 * GU_RB, GU0 = 4 * XH;
constexpr int QK_RB = 6 * 256 + 128, QK_ST = 8 * QK_RB, QK0 = GU0 + 2 * GU_ST;
constexpr int LDS_BYTES = QK0 + 2 * QK_ST;
constexpr unsigned OOB = 0x40000000u;

struct Ctx {
  LAS char* xr; LAS char* gr; LAS char* qr;
  unsigned mX, mG, mQ;
  unsigned vX, vG, vQ;
  rsrc_t sX, sG, sU, sQ, sV;
};

template <int ST> __device__ __forceinline__ void stage_Wa(const Ctx& c, unsigned kG) {
  dma64<GU0 + ST * GU_ST>(c.mG, c.vG, c.sG, kG);
  dma64<GU0 + ST * GU_ST + 1024>(c.mG, c.vG, c.sU, kG);
}
template <int ST> __device__ __forceinline__ void stage_Wb(const Ctx& c, unsigned kG, unsigned kQ) {
  dma48_o128<GU0 + ST * GU_ST + 2048>(c.mG, c.vG, c.sG, kG);
  dma48_o128<GU0 + ST * GU_ST + 2816>(c.mG, c.vG, c.sU, kG);
  dma64<QK0 + ST * QK_ST>(c.mQ, c.vQ, c.sQ, kQ);
  dma32<QK0 + ST * QK_ST + 1024>(c.mQ, c.vQ, c.sV, kQ);
}
template <int ST, int AI> __device__ __forceinline__ void stage_X(const Ctx& c, unsigned kX) {
  dma64<X0 + (ST * 2 + AI) * XH>(c.mX, c.vX, c.sX, kX + AI * 262144u);
  dma64<X0 + (ST * 2 + AI) * XH + 8 * XP>(c.mX, c.vX, c.sX, kX + AI * 262144u + 131072u);
}
template <int ST, int AI> __device__ __forceinline__ void read_X(const Ctx& c, bf16x8 (&xf)[4][2]) {
#pragma unroll
  for (int mt = 0; mt < 4; ++mt)
#pragma unroll
    for (int kk = 0; kk < 2; ++kk) xf[mt][kk] = lds_b128(c.xr + (ST * 2 + AI) * XH + mt * 2 * XP + kk * 64);
}
template <int ST> __device__ __forceinline__ void read_Wa(const Ctx& c, bf16x8 (&wa)[2][2]) {
#pragma unroll
  for (int kk = 0; kk < 2; ++kk) {
    wa[0][kk] = lds_tr2(c.gr + ST * GU_ST + kk * 4 * GU_RB);
    wa[1][kk] = lds_tr2(c.gr + ST * GU_ST + kk * 4 * GU_RB + 1024);
  }
}
template <int ST, bool W3> __device__ __forceinline__ void read_Wb(const Ctx& c, bf16x8 (&wb)[3][2]) {
#pragma unroll
  for (int kk = 0; kk < 2; ++kk) {
    if constexpr (!W3) {
      wb[0][kk] = lds_tr2(c.gr + ST * GU_ST + kk * 4 * GU_RB + 2048);
      wb[1][kk] = lds_tr2(c.gr + ST * GU_ST + kk * 4 * GU_RB + 2816);
      wb[2][kk] = lds_tr2(c.qr + ST * QK_ST + kk * 4 * QK_RB);
    } else {
      wb[0][kk] = lds_tr2(c.qr + ST * QK_ST + kk * 4 * QK_RB);
      wb[1][kk] = lds_tr2(c.qr + ST * QK_ST + kk * 4 * QK_RB + 256);
      wb[2][kk] = lds_tr2(c.qr + ST * QK_ST + kk * 4 * QK_RB + 512);
    }
  }
}
#define PIN_X(xf) asm volatile("" : "+v"(xf[0][0]), "+v"(xf[0][1]), "+v"(xf[1][0]), "+v"(xf[1][1]), "+v"(xf[2][0]), "+v"(xf[2][1]), "+v"(xf[3][0]), "+v"(xf[3][1]))
__device__ __forceinline__ void mma_a(f32x4 (&acc)[4][5], bf16x8 (&xf)[4][2], bf16x8 (&wa)[2][2]) {
  PIN_X(xf);
  __builtin_amdgcn_s_setprio(1);
#pragma unroll
  for (int kk = 0; kk < 2; ++kk)
#pragma unroll
    for (int mt = 0; mt < 4; ++mt)
#pragma unroll
      for (int s = 0; s < 2; ++s) acc[mt][s] = MFMA16(wa[s][kk], xf[mt][kk], acc[mt][s]);
  __builtin_amdgcn_s_setprio(0);
  asm volatile("" : "+v"(acc[0][0]), "+v"(acc[0][1]), "+v"(acc[1][0]), "+v"(acc[1][1]), "+v"(acc[2][0]), "+v"(acc[2][1]), "+v"(acc[3][0]), "+v"(acc[3][1]));
}
template <bool W3> __device__ __forceinline__ void mma_b(f32x4 (&acc)[4][5], bf16x8 (&xf)[4][2], bf16x8 (&wb)[3][2]) {
  PIN_X(xf);
  __builtin_amdgcn_s_setprio(1);
#pragma unroll
  for (int kk = 0; kk < 2; ++kk)
#pragma unroll
    for (int mt = 0; mt < 4; ++mt)
#pragma unroll
      for (int s = 0; s < 3; ++s) {
        if (W3 && s > 0) acc[mt][2 + s] = MFMA16(xf[mt][kk], wb[s][kk], acc[mt][2 + s]);
        else             acc[mt][2 + s] = MFMA16(wb[s][kk], xf[mt][kk], acc[mt][2 + s]);
      }
  __builtin_amdgcn_s_setprio(0);
  asm volatile("" : "+v"(acc[0][2]), "+v"(acc[0][3]), "+v"(acc[0][4]), "+v"(acc[1][2]), "+v"(acc[1][3]), "+v"(acc[1][4]),
                    "+v"(acc[2][2]), "+v"(acc[2][3]), "+v"(acc[2][4]), "+v"(acc[3][2]), "+v"(acc[3][3]), "+v"(acc[3][4]));
}

template <int ST, bool W3>
__device__ __forceinline__ void ktile(const Ctx& c, f32x4 (&acc)[2][4][5], bf16x8 (&xf)[4][2], bf16x8 (&wa)[2][2], bf16x8 (&wb)[3][2],
                                      unsigned kX, unsigned kG, unsigned kQ) {
  read_X<ST, 0>(c, xf); stage_Wa<ST>(c, kG);
  WAIT_VL(14); SCHED(); BAR(); SCHED();
  mma_a(acc[0], xf, wa);
  SCHED(); BAR(); SCHED();
  read_Wb<ST, W3>(c, wb); stage_X<ST, 0>(c, kX);
  WAIT_VL(14); SCHED(); BAR(); SCHED();
  mma_b<W3>(acc[0], xf, wb);
  SCHED(); BAR(); SCHED();
  read_X<ST, 1>(c, xf); stage_Wb<ST>(c, kG, kQ);
  WAIT_VL(16); SCHED(); BAR(); SCHED();
  mma_a(acc[1], xf, wa);
  SCHED(); BAR(); SCHED();
  read_Wa<ST ^ 1>(c, wa); stage_X<ST, 1>(c, kX);
  WAIT_VL(16); SCHED(); BAR(); SCHED();
  mma_b<W3>(acc[1], xf, wb);
  SCHED(); BAR(); SCHED();
}

template <bool W3>
__device__ __forceinline__ void body(const Ctx& c, int wr, int wc, int pm, int j, char* ws) {
  f32x4 acc[2][4][5];
#pragma unroll
  for (int a = 0; a < 2; ++a)
#pragma unroll
    for (int m = 0; m < 4; ++m)
#pragma unroll
      for (int s = 0; s < 5; ++s) acc[a][m][s] = (f32x4){0.f, 0.f, 0.f, 0.f};
  bf16x8 xf[4][2], wa[2][2], wb[3][2];
  STAMP_DECL; STAMP_AT(0);
  stage_Wa<0>(c, 0); stage_X<0, 0>(c, 0); stage_Wb<0>(c, 0, 0); stage_X<0, 1>(c, 0);
  stage_Wa<1>(c, 64u * 7168u); stage_X<1, 0>(c, 128); stage_Wb<1>(c, 64u * 7168u, 64u * 6144u); stage_X<1, 1>(c, 128);
  WAIT_V(16); BAR();
  if (wr == 1) BAR();
  read_Wa<0>(c, wa);
  WAIT_L0(); SCHED(); BAR(); SCHED(); BAR(); SCHED();
  STAMP_AT(1);
#pragma unroll 1
  for (int t = 0; t < 16; t += 2) {
    if (t == 2) STAMP_AT(2);
    const bool more = t < 14;
    unsigned kX = more ? (unsigned)(t + 2) * 128u : OOB;
    unsigned kG = more ? (unsigned)(t + 2) * 64u * 7168u : OOB;
    unsigned kQ = more ? (unsigned)(t + 2) * 64u * 6144u : OOB;
    ktile<0, W3>(c, acc, xf, wa, wb, kX, kG, kQ);
    ktile<1, W3>(c, acc, xf, wa, wb, kX + 128u, kG + 64u * 7168u, kQ + 64u * 6144u);
  }
  if (wr == 0) BAR();
  STAMP_AT(3);
  int tz = threadIdx.x; asm volatile("" : "+v"(tz));
  const int lane = tz & 63, fr = lane & 15, fq = lane >> 4;
  const int row0 = pm * 256 + wr * 64 + fr;
  char* a2 = ws + WS_A2;
  {
    char* p0 = a2 + ((size_t)(j * 7 + wc) * 2048 + row0) * 32 + fq * 8;
#pragma unroll
    for (int a = 0; a < 2; ++a)
#pragma unroll
      for (int m = 0; m < 4; ++m) {
        const f32x4 g = acc[a][m][0], u = acc[a][m][1];
        u32x2 w; w.x = pk_bf16(geglu(g[0], u[0]), geglu(g[1], u[1])); w.y = pk_bf16(geglu(g[2], u[2]), geglu(g[3], u[3]));
        *(u32x2*)(p0 + (a * 128 + m * 16) * 32) = w;
        if constexpr (!W3) {
          const f32x4 g2 = acc[a][m][2], u2 = acc[a][m][3];
          u32x2 w2; w2.x = pk_bf16(geglu(g2[0], u2[0]), geglu(g2[1], u2[1])); w2.y = pk_bf16(geglu(g2[2], u2[2]), geglu(g2[3], u2[3]));
          *(u32x2*)(p0 + (size_t)4 * 2048 * 32 + (a * 128 + m * 16) * 32) = w2;
        }
      }
  }
  const int qk_off = (j * 64 + pm * 8 + wr * 2) * 2048 + (fq >> 1) * 512 + fr * 16 + (fq & 1) * 8;
  if constexpr (!W3) {
    char* p = ws + (wc == 2 ? WS_K : WS_Q) + qk_off + (wc & 1) * 1024;
#pragma unroll
    for (int a = 0; a < 2; ++a)
#pragma unroll
      for (int m = 0; m < 4; ++m) {
        const f32x4 v = acc[a][m][4];
        u32x2 w; w.x = pk_bf16(v[0], v[1]); w.y = pk_bf16(v[2], v[3]);
        *(u32x2*)(p + (a * 4 + (m >> 1)) * 2048 + (m & 1) * 256) = w;
      }
  } else {
    char* pk = ws + WS_K + qk_off + 1024;
    const int kg0 = pm * 16 + wr * 4;
    char* pv = ws + WS_VT + (size_t)(j * 128 + kg0) * 1024 + (fq & 1) * 512 + fr * 16 + (fq >> 1) * 8;
#pragma unroll
    for (int a = 0; a < 2; ++a)
#pragma unroll
      for (int m = 0; m < 4; ++m) {
        const f32x4 v = acc[a][m][2], v0 = acc[a][m][3], v1 = acc[a][m][4];
        u32x2 w; w.x = pk_bf16(v[0], v[1]); w.y = pk_bf16(v[2], v[3]);
        *(u32x2*)(pk + (a * 4 + (m >> 1)) * 2048 + (m & 1) * 256) = w;
        u32x2 w0; w0.x = pk_bf16(v0[0], v0[1]); w0.y = pk_bf16(v0[2], v0[3]);
        u32x2 w1; w1.x = pk_bf16(v1[0], v1[1]); w1.y = pk_bf16(v1[2], v1[3]);
        *(u32x2*)(pv + (a * 8 + m) * 1024) = w0;
        *(u32x2*)(pv + (a * 8 + m) * 1024 + 256) = w1;
      }
  }
  STAMP_AT(4);
  STAMP_AT(5);
  STAMP_FLUSH(36u << 20, 6);
}
}

__global__ __launch_bounds__(512, 2) void k1_gemm(const u16* __restrict__ x, const u16* __restrict__ wqkv, const u16* __restrict__ wg,
                                                  const u16* __restrict__ wu, const u16* __restrict__ gw, char* __restrict__ ws) {
  using namespace k1;
  extern __shared__ __attribute__((aligned(16))) char smem[];
  LAS char* lds = (LAS char*)smem;
  const unsigned lbase = (unsigned)(size_t)lds;
  const int tid = threadIdx.x, lane = tid & 63;
  const int wid = __builtin_amdgcn_readfirstlane(tid >> 6), wr = wid >> 2, wc = wid & 3;
  const int bid = blockIdx.x, xcd = bid & 7, idx = bid >> 3;
  const int j = xcd * 4 + (idx >> 3), pm = idx & 7;
  Ctx c;
  c.sX = mk_rsrc(x, S_ * D_ * 2);
  c.sG = mk_rsrc(wg, D_ * F_ * 2);
  c.sU = mk_rsrc(wu, D_ * F_ * 2);
  c.sQ = mk_rsrc(wqkv, D_ * 3072 * 2);
  c.sV = mk_rsrc((const char*)wqkv + 4096, D_ * 3072 * 2 - 4096);
  {
    const int r = lane >> 3, cp = lane & 7, ch = (cp & 4) | ((cp & 3) ^ ((r >> 1) & 3));
    c.vX = (unsigned)((pm * 256 + wid * 8 + r) * 2048 + ch * 16);
    const int u = lane >> 4, rr = (lane >> 1) & 7, hf = lane & 1;
    c.vG = (unsigned)((wid * 8 + rr) * 7168 + j * 224 + u * 32 + hf * 16);
    c.vQ = (unsigned)((wid * 8 + rr) * 6144 + (u >> 1) * 2048 + j * 64 + (u & 1) * 32 + hf * 16);
    c.mX = lbase + wid * XP; c.mG = lbase + wid * GU_RB; c.mQ = lbase + wid * QK_RB;
    const int fr = lane & 15, q = lane >> 4;
    c.xr = lds + X0 + (wr * 8 + (fr >> 3)) * XP + (fr & 7) * 128 + ((q ^ ((fr >> 1) & 3)) * 16);
    const int qq = (lane & 15) >> 2, p = lane & 3;
    c.gr = lds + GU0 + q * GU_RB + qq * 32 + p * 8 + wc * 256;
    c.qr = lds + QK0 + q * QK_RB + qq * 32 + p * 8 + wc * 256;
  }
  if (wc == 3) body<true>(c, wr, wc, pm, j, ws); else body<false>(c, wr, wc, pm, j, ws);
  {
    const int row = pm * 256 + j * 8 + wid;
    const u32x4* xp = (const u32x4*)(x + (size_t)row * D_) + lane * 2;
    const u32x4* gp = (const u32x4*)gw + lane * 2;
    float s = 0.f;
#pragma unroll
    for (int i = 0; i < 2; ++i) {
      const u32x4 a = xp[i], b = gp[i];
#pragma unroll
      for (int e = 0; e < 4; ++e) s += bf_lo(a[e]) * bf_lo(b[e]) + bf_hi(a[e]) * bf_hi(b[e]);
    }
#pragma unroll
    for (int o = 32; o > 0; o >>= 1) s += __shfl_xor(s, o);
    if (lane == 0) ((float*)(ws + WS_G))[row] = 1.0f / (1.0f + __expf(-s));
    if (bid == 0 && tid < 136) ((unsigned*)(ws + WS_FLAG))[tid * 64] = 0u;
  }
}

#define KX_TILES 16
namespace k2 {
constexpr int B_RB = 1024 + 32, B_H = 8 * B_RB, ST_A = 16384, ST_B = 2 * B_H, ST = ST_A + ST_B, NST = 4, LDS_BYTES = NST * ST;
constexpr int NT_DOWN = 56, NT_WO = 16, KX = KX_TILES;
constexpr unsigned OOB = 0x40000000u;
__device__ __forceinline__ bf16x8 lds_tr2b(LAS char* p) {
  s16x4 a = __builtin_amdgcn_ds_read_tr16_b64_v4i16((LAS s16x4*)p);
  s16x4 b = __builtin_amdgcn_ds_read_tr16_b64_v4i16((LAS s16x4*)(p + 512));
  s16x8 r = __builtin_shufflevector(a, b, 0, 1, 2, 3, 4, 5, 6, 7);
  return __builtin_bit_cast(bf16x8, r);
}
__device__ __forceinline__ void wait_flag(const unsigned* fl, unsigned need) {
  unsigned spins = 0;
  while ((unsigned)__builtin_amdgcn_readfirstlane(__hip_atomic_load(fl, __ATOMIC_RELAXED, __HIP_MEMORY_SCOPE_AGENT)) < need) {
    __builtin_amdgcn_s_sleep(2);
    if (++spins > (1u << 22)) break;
  }
  __builtin_amdgcn_fence(__ATOMIC_ACQUIRE, "agent");
  asm volatile("s_waitcnt vmcnt(0)" ::: "memory");
}
template <int I0, int NT>
__device__ __forceinline__ void attn_chunk(LAS char* kl, LAS char* vl, bf16x8 qf0, bf16x8 qf1, int qt, int l31, int hh, float& mrun, f32x2& lrun, f32x16& oacc) {
  const float NEG = -__builtin_inff();
  const float cs = 0.17677669529663687f * 1.4426950408889634f;
  f32x16 sc[NT];
#pragma unroll
  for (int i = 0; i < NT; ++i) {
    f32x16 z = {};
    z = MFMA32(lds_b128(kl + (I0 + i) * 2048), qf0, z);
    sc[i] = MFMA32(lds_b128(kl + (I0 + i) * 2048 + 1024), qf1, z);
  }
#pragma unroll
  for (int r = 0; r < 16; ++r) {
    const int kl_ = (r & 3) + 8 * (r >> 2) + 4 * hh;
    if constexpr (I0 == 0) sc[0][r] = (kl_ > l31) ? sc[0][r] : NEG;
    if constexpr (I0 + NT == 9) sc[NT - 1][r] = (kl_ <= l31) ? sc[NT - 1][r] : NEG;
  }
  float mx = mrun;
#pragma unroll
  for (int i = 0; i < NT; ++i) {
    float ma = fmaxf(sc[i][0], sc[i][1]), mb = fmaxf(sc[i][2], sc[i][3]);
#pragma unroll
    for (int r = 4; r < 16; r += 4) { ma = fmaxf(fmaxf(ma, sc[i][r]), sc[i][r + 1]); mb = fmaxf(fmaxf(mb, sc[i][r + 2]), sc[i][r + 3]); }
    const float bias = (qt - 8 + I0 + i < 0) ? NEG : 0.f;
    mx = fmaxf(mx, fmaxf(ma, mb) + bias);
  }
  mx = fmaxf(mx, __shfl_xor(mx, 32));
  const float mxs = fmaxf(mx, -3.0e38f);
  if constexpr (I0 != 0) {
    const float al = __builtin_amdgcn_exp2f((mrun - mxs) * cs);
    lrun *= al;
#pragma unroll
    for (int r = 0; r < 16; ++r) oacc[r] *= al;
  }
  mrun = mx;
  const f32x2 cs2 = {cs, cs};
  const float mc = -mxs * cs;
  f32x2 l2[2] = {lrun, {0.f, 0.f}};
#pragma unroll
  for (int i = 0; i < NT; ++i) {
    const float mci = mc + ((qt - 8 + I0 + i < 0) ? NEG : 0.f);
    const f32x2 mc2 = {mci, mci};
#pragma unroll
    for (int s = 0; s < 2; ++s) {
      u32x4 pw;
#pragma unroll
      for (int e = 0; e < 4; ++e) {
        const f32x2 sv = {sc[i][8 * s + 2 * e], sc[i][8 * s + 2 * e + 1]};
        const f32x2 t = __builtin_elementwise_fma(sv, cs2, mc2);
        f32x2 p; p.x = __builtin_amdgcn_exp2f(t.x); p.y = __builtin_amdgcn_exp2f(t.y);
        l2[e & 1] += p;
        pw[e] = pk_bf16(p.x, p.y);
      }
      oacc = MFMA32(lds_b128(vl + (I0 + i) * 2048 + s * 1024), __builtin_bit_cast(bf16x8, pw), oacc);
    }
  }
  lrun = l2[0] + l2[1];
}
__device__ __forceinline__ void attn_head(LAS char* kl, LAS char* vl, bf16x8 qf0, bf16x8 qf1, int qt, int lane, rsrc_t sA, int head, float gq) {
  const int l31 = lane & 31, hh = lane >> 5;
  float mrun = -__builtin_inff(); f32x2 lrun = {0.f, 0.f}; f32x16 oacc = {};
  attn_chunk<0, 5>(kl, vl, qf0, qf1, qt, l31, hh, mrun, lrun, oacc);
  asm volatile("" : "+v"(oacc), "+v"(lrun), "+v"(mrun), "+v"(qf0), "+v"(qf1)); SCHED();
  attn_chunk<5, 4>(kl, vl, qf0, qf1, qt, l31, hh, mrun, lrun, oacc);
  float sum = lrun.x + lrun.y;
  sum += __shfl_xor(sum, 32);
  const float inv = gq / sum;
#pragma unroll
  for (int g2 = 0; g2 < 2; ++g2) {
    unsigned a0 = pk_bf16(oacc[8 * g2 + 0] * inv, oacc[8 * g2 + 1] * inv), a1 = pk_bf16(oacc[8 * g2 + 2] * inv, oacc[8 * g2 + 3] * inv);
    unsigned b0 = pk_bf16(oacc[8 * g2 + 4] * inv, oacc[8 * g2 + 5] * inv), b1 = pk_bf16(oacc[8 * g2 + 6] * inv, oacc[8 * g2 + 7] * inv);
    auto r0 = __builtin_amdgcn_permlane32_swap(a0, b0, false, false);
    auto r1 = __builtin_amdgcn_permlane32_swap(a1, b1, false, false);
    u32x4 w; w.x = r0[0]; w.y = r1[0]; w.z = r0[1]; w.w = r1[1];
    __builtin_amdgcn_raw_buffer_store_b128(w, sA, (unsigned)(((224 + head * 2 + g2) * 2048 + qt * 32 + l31) * 32 + hh * 16), 0, 16);
  }
}

template <int MC> __device__ __forceinline__ void dma4k(unsigned mbase, unsigned voff, rsrc_t srd, unsigned soff) {
  asm volatile("s_add_u32 m0, %0, %4\n\ts_nop 0\n\tbuffer_load_dwordx4 %1, %2, %3 offen lds\n\tbuffer_load_dwordx4 %1, %2, %3 offen offset:1024 lds\n\t"
               "buffer_load_dwordx4 %1, %2, %3 offen offset:2048 lds\n\tbuffer_load_dwordx4 %1, %2, %3 offen offset:3072 lds"
               :: "s"(mbase), "v"(voff), "s"(srd), "s"(soff), "i"(MC) : "memory", "scc");
}
struct LCtx { unsigned vA, vB0, vB1, mA, mB; rsrc_t sA; };
template <int SL> __device__ __forceinline__ void stage(const LCtx& c, rsrc_t sB, unsigned ga, unsigned kb) {
  dma4k<SL * ST>(c.mA, c.vA, c.sA, ga);
  dma64<SL * ST>(c.mB, c.vB0, sB, kb); dma64_o128<SL * ST + B_H>(c.mB, c.vB0, sB, kb);
  dma64<SL * ST + B_RB>(c.mB, c.vB1, sB, kb); dma64_o128<SL * ST + B_RB + B_H>(c.mB, c.vB1, sB, kb);
}
struct Ctx { LAS char* ar; LAS char* br0; LAS char* br1; LAS char* ar2; LAS char* br02; LAS char* br12; };
template <int SL, int M>
__device__ __forceinline__ void quarter(const Ctx& c, f32x4 (&acc)[4][4], bf16x8 (&af)[4], bf16x8 (&bf)[4], bf16x8 (&an)[4], bf16x8 (&bn)[4]) {
  constexpr int ns = (SL + 1) & 3, so = (ns & 1) * ST;
  LAS char* const ar = (ns & 2) ? c.ar2 : c.ar; LAS char* const br0 = (ns & 2) ? c.br02 : c.br0; LAS char* const br1 = (ns & 2) ? c.br12 : c.br1;
  asm volatile("" : "+v"(af[M]));
#pragma unroll
  for (int n = 0; n < 4; ++n) acc[M][n] = MFMA16(bf[n], af[M], acc[M][n]);
  asm volatile("" : "+v"(acc[M][0]), "+v"(acc[M][1]), "+v"(acc[M][2]), "+v"(acc[M][3]));
  SCHED();
  if constexpr (M < 2) {
    bn[2 * M] = lds_tr2b((M ? br1 : br0) + so); bn[2 * M + 1] = lds_tr2b((M ? br1 : br0) + so + 32);
    an[2 * M] = lds_b128(ar + so + 2 * M * 512);
  }
  if constexpr (M == 2) { an[1] = lds_b128(ar + so + 512); an[3] = lds_b128(ar + so + 3 * 512); }
  SCHED();
}
template <int SL>
__device__ __forceinline__ void step(const Ctx& c, f32x4 (&acc)[4][4], bf16x8 (&af)[4], bf16x8 (&bf)[4], bf16x8 (&an)[4], bf16x8 (&bn)[4]) {
  quarter<SL, 0>(c, acc, af, bf, an, bn); quarter<SL, 1>(c, acc, af, bf, an, bn);
  quarter<SL, 2>(c, acc, af, bf, an, bn); quarter<SL, 3>(c, acc, af, bf, an, bn);
  WAIT_L0(); SCHED(); BAR(); SCHED();
}
}

__global__ __launch_bounds__(768, 3) void k2_gemm(const u16* __restrict__ wo, const u16* __restrict__ wd, char* __restrict__ ws, u16* __restrict__ out) {
  using namespace k2;
  extern __shared__ __attribute__((aligned(16))) char smem[];
  LAS char* lds = (LAS char*)smem;
  const unsigned lbase = (unsigned)(size_t)lds;
  const int tid = threadIdx.x, lane = tid & 63;
  const int wid = __builtin_amdgcn_readfirstlane(tid >> 6);
  const int bid = blockIdx.x, xcd = bid & 7, idx = bid >> 3;
  const bool roleX = (idx & 1) != 0;
  const int mt = (xcd >> 1) * 4 + (idx >> 3), nt = (xcd & 1) * 4 + ((idx >> 1) & 3);
  const int panel = mt >> 1, tile = mt * 8 + nt;
  unsigned* flags = (unsigned*)(ws + WS_FLAG);
  const int nq = roleX ? KX / 4 : (NT_DOWN - KX + NT_WO) / 4, q_sw = roleX ? 1000 : (NT_DOWN - KX) / 4 - 1;
  const int head0 = 2 * ((mt & 1) * 8 + nt);

  if (wid >= 8) {
    const int l = wid - 8;
    rsrc_t sB = mk_rsrc(wd, roleX ? KX * 131072 : F_ * D_ * 2);
    const rsrc_t sO = mk_rsrc(wo, D_ * D_ * 2);
    LCtx c;
    c.sA = mk_rsrc(ws + WS_A2, roleX ? KX * 262144 : A2_BYTES);
    c.vA = (unsigned)((l * 2048 + mt * 128) * 32 + lane * 16);
    c.vB0 = (unsigned)((l * 16 + (lane >> 3)) * 2048 + nt * 256 + (((lane & 7) ^ ((lane >> 2) & 4)) * 16)); c.vB1 = c.vB0 + 8u * 2048u;
    c.mA = lbase + l * 4096; c.mB = lbase + ST_A + l * 2 * B_RB;
    unsigned ga = roleX ? 0u : KX * 262144u, kb = roleX ? 0u : KX * 131072u;
    if (roleX) {
      const rsrc_t sK = mk_rsrc(ws + WS_K, 4u << 20), sV = mk_rsrc(ws + WS_VT, 4u << 20);
      const unsigned so = (panel == 0 && l < 2) ? OOB : (unsigned)(head0 * 131072 + panel * 16384 - 16384 + l * 8192);
      const unsigned vl = lane * 16, mk = lbase + l * 8192;
      dma4k<0>(mk, vl, sK, so); dma4k<4096>(mk, vl, sK, so + 4096); dma4k<32768>(mk, vl, sV, so); dma4k<32768 + 4096>(mk, vl, sV, so + 4096);
      const unsigned s1 = so + 131072;
      dma4k<2 * ST>(mk, vl, sK, s1); dma4k<2 * ST + 4096>(mk, vl, sK, s1 + 4096); dma4k<2 * ST + 32768>(mk, vl, sV, s1); dma4k<2 * ST + 32768 + 4096>(mk, vl, sV, s1 + 4096);
      WAIT_V(16); BAR();
      WAIT_V(0); BAR();
      stage<0>(c, sB, ga, kb); stage<1>(c, sB, ga + 262144u, kb + 131072u);
      BAR();
    } else {
      stage<0>(c, sB, ga, kb); stage<1>(c, sB, ga + 262144u, kb + 131072u);
    }
    stage<2>(c, sB, ga + 2u * 262144u, kb + 2u * 131072u); stage<3>(c, sB, ga + 3u * 262144u, kb + 3u * 131072u);
    ga += 4u * 262144u; kb += 4u * 131072u;
    WAIT_V(16); BAR();
    BAR();
#pragma unroll 1
    for (int q = 0; q < nq; ++q) {
      if (q == q_sw - 1 && l == 0) wait_flag(flags + panel * 64, 16u);
      if (q == q_sw) { sB = sO; kb = 0; }
      stage<0>(c, sB, ga, kb); WAIT_V(16); BAR();
      stage<1>(c, sB, ga + 262144u, kb + 131072u); WAIT_V(16); BAR();
      stage<2>(c, sB, ga + 2u * 262144u, kb + 2u * 131072u); WAIT_V(16); BAR();
      stage<3>(c, sB, ga + 3u * 262144u, kb + 3u * 131072u); WAIT_V(16); BAR();
      ga += 4u * 262144u; kb += 4u * 131072u;
    }
    WAIT_V(0);
    if (!roleX && l == 0) wait_flag(flags + (8 + tile) * 64, 1u);
    BAR();
    return;
  }

  const int kg = wid >> 2, wm = (wid >> 1) & 1, wn = wid & 1;
  const int fr = lane & 15, fq = lane >> 4;
  STAMP_DECL; STAMP_AT(0);
  if (roleX) {
    const int qt = panel * 8 + wid;
    const rsrc_t sA = mk_rsrc(ws + WS_A2, A2_BYTES);
    const char* qp = ws + WS_Q + (size_t)(head0 * 64 + qt) * 2048 + lane * 16;
    const bf16x8 qa0 = *(const bf16x8*)qp, qa1 = *(const bf16x8*)(qp + 1024);
    const float gq = ((const float*)(ws + WS_G))[qt * 32 + (lane & 31)];
    BAR();
    STAMP_AT(1);
    attn_head(lds + wid * 2048 + lane * 16, lds + 32768 + wid * 2048 + lane * 16, qa0, qa1, qt, lane, sA, head0, gq);
    const bf16x8 qb0 = *(const bf16x8*)(qp + 131072), qb1 = *(const bf16x8*)(qp + 131072 + 1024);
    WAIT_L0(); BAR();
    attn_head(lds + 2 * ST + wid * 2048 + lane * 16, lds + 2 * ST + 32768 + wid * 2048 + lane * 16, qb0, qb1, qt, lane, sA, head0 + 1, gq);
    WAIT_VL(0); BAR();
    if (tid == 0) __hip_atomic_fetch_add(flags + panel * 64, 1u, __ATOMIC_RELAXED, __HIP_MEMORY_SCOPE_AGENT);
  } else {
    STAMP_AT(1);
  }
  STAMP_AT(2);
  Ctx c;
  c.ar = lds + (kg * 2 + (fq >> 1)) * 4096 + (wm * 64 + fr) * 32 + (fq & 1) * 16;
  c.br0 = lds + ST_A + wn * B_H + (kg * 4 + fq) * B_RB + (fr >> 2) * 128 + ((fr >> 3) * 64) + (lane & 3) * 8;
  c.br1 = lds + ST_A + wn * B_H + (kg * 4 + fq) * B_RB + (fr >> 2) * 128 + (((fr >> 3) ^ 1) * 64) + (lane & 3) * 8;
  c.ar2 = c.ar + 2 * ST; c.br02 = c.br0 + 2 * ST; c.br12 = c.br1 + 2 * ST;
  asm volatile("" : "+v"(c.ar2), "+v"(c.br02), "+v"(c.br12));
  float og[4];
  { const float* gptr = (const float*)(ws + WS_G) + mt * 128 + wm * 64 + fr;
#pragma unroll
    for (int m = 0; m < 4; ++m) og[m] = 1.0f - gptr[m * 16];
    asm volatile("" : "+v"(og[0]), "+v"(og[1]), "+v"(og[2]), "+v"(og[3])); }
  f32x4 acc[4][4];
#pragma unroll
  for (int m = 0; m < 4; ++m)
#pragma unroll
    for (int n = 0; n < 4; ++n) acc[m][n] = (f32x4){0.f, 0.f, 0.f, 0.f};
  BAR();
  STAMP_AT(3);
  {
    bf16x8 af0[4], bf0[4], af1[4], bf1[4];
    bf0[0] = lds_tr2b(c.br0); bf0[1] = lds_tr2b(c.br0 + 32); bf0[2] = lds_tr2b(c.br1); bf0[3] = lds_tr2b(c.br1 + 32);
#pragma unroll
    for (int m = 0; m < 4; ++m) af0[m] = lds_b128(c.ar + m * 512);
    WAIT_L0(); SCHED(); BAR(); SCHED();
#pragma unroll 1
    for (int q = 0; q < nq; ++q) {
      if (q == q_sw + 1) {
#pragma unroll
        for (int m = 0; m < 4; ++m)
#pragma unroll
          for (int n = 0; n < 4; ++n) acc[m][n] *= og[m];
      }
      step<0>(c, acc, af0, bf0, af1, bf1);
      step<1>(c, acc, af1, bf1, af0, bf0);
      step<2>(c, acc, af0, bf0, af1, bf1);
      step<3>(c, acc, af1, bf1, af0, bf0);
    }
  }
  STAMP_AT(4);
  BAR();
  STAMP_AT(5);
  int tz = threadIdx.x; asm volatile("" : "+v"(tz));
  const int ln = tz & 63, fr2 = ln & 15, fq2 = ln >> 4;
  const float gv[2] = {kg ? og[2] : og[0], kg ? og[3] : og[1]};
  const rsrc_t sP = mk_rsrc(ws + WS_PART + (size_t)tile * 65536, 65536);
  f32x4 pp[2][4];
  if (!roleX) {
#pragma unroll
    for (int m = 0; m < 2; ++m)
#pragma unroll
      for (int n = 0; n < 4; ++n)
        pp[m][n] = __builtin_bit_cast(f32x4, __builtin_amdgcn_raw_buffer_load_b128(sP, (unsigned)(ln * 16), (unsigned)((wid * 8 + m * 4 + n) * 1024), 0));
  }
  LAS f32x4* red = (LAS f32x4*)lds + ln;
  f32x4 r[2][4];
  if (kg == 0) {
#pragma unroll
    for (int m = 0; m < 2; ++m)
#pragma unroll
      for (int n = 0; n < 4; ++n) { red[(wid * 8 + m * 4 + n) * 64] = acc[2 + m][n]; r[m][n] = acc[m][n]; }
  } else {
#pragma unroll
    for (int m = 0; m < 2; ++m)
#pragma unroll
      for (int n = 0; n < 4; ++n) { red[(wid * 8 + m * 4 + n) * 64] = acc[m][n]; r[m][n] = acc[2 + m][n]; }
  }
  WAIT_L0(); BAR();
#pragma unroll
  for (int m = 0; m < 2; ++m)
#pragma unroll
    for (int n = 0; n < 4; ++n) r[m][n] += red[((wid ^ 4) * 8 + m * 4 + n) * 64];
  if (roleX) {
#pragma unroll
    for (int m = 0; m < 2; ++m)
#pragma unroll
      for (int n = 0; n < 4; ++n)
        __builtin_amdgcn_raw_buffer_store_b128(__builtin_bit_cast(u32x4, r[m][n]), sP, (unsigned)(ln * 16), (unsigned)((wid * 8 + m * 4 + n) * 1024), 16);
    asm volatile("s_waitcnt vmcnt(0)" ::: "memory");
    BAR();
    if (tid == 0) __hip_atomic_store(flags + (8 + tile) * 64, 1u, __ATOMIC_RELAXED, __HIP_MEMORY_SCOPE_AGENT);
  } else {
    u16* op = out + (size_t)(mt * 128 + wm * 64 + kg * 32 + fr2) * D_ + nt * 128 + wn * 64 + fq2 * 4;
#pragma unroll
    for (int m = 0; m < 2; ++m) {
#pragma unroll
      for (int n = 0; n < 4; ++n) {
        const f32x4 v = r[m][n] + pp[m][n] * gv[m];
        u32x2 w; w.x = pk_bf16(v[0], v[1]); w.y = pk_bf16(v[2], v[3]);
        *(u32x2*)(op + m * 16 * D_ + n * 16) = w;
      }
    }
  }
  STAMP_AT(6);
  STAMP_FLUSH(37u << 20, 7);
}

extern "C" void kernel_launch(void* const* d_in, const int* in_sizes, int n_in, void* d_out, int out_size, void* d_ws, size_t ws_size,
                              hipStream_t stream) {
  const u16* x = (const u16*)d_in[0]; const u16* wqkv = (const u16*)d_in[1]; const u16* wo = (const u16*)d_in[2];
  const u16* wg = (const u16*)d_in[4]; const u16* wu = (const u16*)d_in[5]; const u16* wd = (const u16*)d_in[6]; const u16* gw = (const u16*)d_in[7];
  (void)hipFuncSetAttribute((const void*)k1_gemm, hipFuncAttributeMaxDynamicSharedMemorySize, k1::LDS_BYTES);
  (void)hipFuncSetAttribute((const void*)k2_gemm, hipFuncAttributeMaxDynamicSharedMemorySize, k2::LDS_BYTES);
  k1_gemm<<<256, 512, k1::LDS_BYTES, stream>>>(x, wqkv, wg, wu, gw, (char*)d_ws);
  k2_gemm<<<256, 768, k2::LDS_BYTES, stream>>>(wo, wd, (char*)d_ws, (u16*)d_out);
}
```

```cpp
#include <hip/hip_runtime.h>
#include <hip/hip_bf16.h>

#define LAS __attribute__((address_space(3)))
typedef unsigned short u16;
typedef short s16x4 __attribute__((ext_vector_type(4)));
typedef short s16x8 __attribute__((ext_vector_type(8)));
typedef __bf16 bf16x8 __attribute__((ext_vector_type(8)));
typedef __bf16 bf16x2 __attribute__((ext_vector_type(2)));
typedef float f32x2 __attribute__((ext_vector_type(2)));
typedef float f32x4 __attribute__((ext_vector_type(4)));
typedef float f32x16 __attribute__((ext_vector_type(16)));
typedef unsigned u32x2 __attribute__((ext_vector_type(2)));
typedef unsigned u32x4 __attribute__((ext_vector_type(4)));
typedef __amdgpu_buffer_rsrc_t rsrc_t;

constexpr int S_ = 2048, D_ = 1024, F_ = 3584;
constexpr size_t WS_A2 = 0;
constexpr size_t WS_Q  = 20u << 20;
constexpr size_t WS_K  = 24u << 20;
constexpr size_t WS_VT = 28u << 20;
constexpr size_t WS_G  = 32u << 20;
constexpr size_t WS_FLAG = 33u << 20;
constexpr size_t WS_PART = 40u << 20;
constexpr unsigned A2_BYTES = 288u * 2048u * 32u;

#define STAMP_DECL
#define STAMP_AT(i)
#define STAMP_FLUSH(base, n)
#define BAR() do { asm volatile("" ::: "memory"); __builtin_amdgcn_s_barrier(); asm volatile("" ::: "memory"); } while (0)
#define SCHED() __builtin_amdgcn_sched_barrier(0)
#define WAIT_VL(n) do { asm volatile("s_waitcnt vmcnt(" #n ")" ::: "memory"); __builtin_amdgcn_s_waitcnt(0xC07F); } while (0)
#define WAIT_V(n) asm volatile("s_waitcnt vmcnt(" #n ")" ::: "memory")
#define WAIT_L0() do { asm volatile("" ::: "memory"); __builtin_amdgcn_s_waitcnt(0xC07F); } while (0)

__device__ __forceinline__ rsrc_t mk_rsrc(const void* p, unsigned bytes) {
  return __builtin_amdgcn_make_buffer_rsrc((void*)p, (short)0, (int)bytes, 0x00020000);
}
template <int MC> __device__ __forceinline__ void dma64(unsigned mbase, unsigned voff, rsrc_t srd, unsigned soff) {
  asm volatile("s_add_u32 m0, %0, %4\n\ts_nop 0\n\tbuffer_load_dwordx4 %1, %2, %3 offen lds"
               :: "s"(mbase), "v"(voff), "s"(srd), "s"(soff), "i"(MC) : "memory", "scc");
}
template <int MC> __device__ __forceinline__ void dma64_o128(unsigned mbase, unsigned voff, rsrc_t srd, unsigned soff) {
  asm volatile("s_add_u32 m0, %0, %4\n\ts_nop 0\n\tbuffer_load_dwordx4 %1, %2, %3 offen offset:128 lds"
               :: "s"(mbase), "v"(voff), "s"(srd), "s"(soff), "i"(MC - 128) : "memory", "scc");
}
template <int MC> __device__ __forceinline__ void dma48_o128(unsigned mbase, unsigned voff, rsrc_t srd, unsigned soff) {
  asm volatile("s_add_u32 m0, %0, %4\n\ts_mov_b32 exec_hi, 0xffff\n\tbuffer_load_dwordx4 %1, %2, %3 offen offset:128 lds\n\ts_mov_b32 exec_hi, -1"
               :: "s"(mbase), "v"(voff), "s"(srd), "s"(soff), "i"(MC - 128) : "memory", "scc");
}
template <int MC> __device__ __forceinline__ void dma32(unsigned mbase, unsigned voff, rsrc_t srd, unsigned soff) {
  asm volatile("s_add_u32 m0, %0, %4\n\ts_mov_b32 exec_hi, 0\n\tbuffer_load_dwordx4 %1, %2, %3 offen lds\n\ts_mov_b32 exec_hi, -1"
               :: "s"(mbase), "v"(voff), "s"(srd), "s"(soff), "i"(MC) : "memory", "scc");
}
__device__ __forceinline__ void touch(unsigned mscr, unsigned voff, rsrc_t srd) {
  asm volatile("s_mov_b32 m0, %0\n\ts_nop 0\n\tbuffer_load_dword %1, %2, 0 offen lds" :: "s"(mscr), "v"(voff), "s"(srd) : "memory");
}
__device__ __forceinline__ void touch2(unsigned mscr, unsigned voff, rsrc_t srd, unsigned soff) {
  asm volatile("s_mov_b32 m0, %0\n\ts_nop 0\n\tbuffer_load_dword %1, %2, %3 offen lds" :: "s"(mscr), "v"(voff), "s"(srd), "s"(soff) : "memory");
}
__device__ __forceinline__ bf16x8 lds_b128(LAS char* p) { return *(LAS bf16x8*)p; }
__device__ __forceinline__ bf16x8 lds_tr2(LAS char* p) {
  s16x4 a = __builtin_amdgcn_ds_read_tr16_b64_v4i16((LAS s16x4*)p);
  s16x4 b = __builtin_amdgcn_ds_read_tr16_b64_v4i16((LAS s16x4*)(p + 128));
  s16x8 r = __builtin_shufflevector(a, b, 0, 1, 2, 3, 4, 5, 6, 7);
  return __builtin_bit_cast(bf16x8, r);
}
__device__ __forceinline__ unsigned pk_bf16(float lo, float hi) {
  bf16x2 v; v.x = (__bf16)lo; v.y = (__bf16)hi; return __builtin_bit_cast(unsigned, v);
}
__device__ __forceinline__ float bf_lo(unsigned u) { return __uint_as_float(u << 16); }
__device__ __forceinline__ float bf_hi(unsigned u) { return __uint_as_float(u & 0xffff0000u); }
#define MFMA16(a, b, c) __builtin_amdgcn_mfma_f32_16x16x32_bf16((a), (b), (c), 0, 0, 0)
#define MFMA32(a, b, c) __builtin_amdgcn_mfma_f32_32x32x16_bf16((a), (b), (c), 0, 0, 0)

__device__ __forceinline__ float geglu(float g, float u) {
  const float c1 = -2.0f * 0.7978845608028654f * 1.4426950408889634f;
  const float c2 = c1 * 0.044715f;
  float z = g * (c1 + c2 * g * g);
  float e = __builtin_amdgcn_exp2f(z);
  return g * u * __builtin_amdgcn_rcpf(1.0f + e);
}

#define PDI 2
namespace k1 {
constexpr int XP = 1088, XH = 16 * XP;
constexpr int X0 = 0;
constexpr int GU_RB = 14 * 256 + 128, GU_ST = 8 * GU_RB, GU0 = 4 * XH;
constexpr int QK_RB = 6 * 256 + 128, QK_ST = 8 * QK_RB, QK0 = GU0 + 2 * GU_ST;
constexpr int SCR = QK0 + 2 * QK_ST, LDS_BYTES = SCR + 256;
constexpr unsigned OOB = 0x40000000u;

struct Ctx {
  LAS char* xr; LAS char* gr; LAS char* qr;
  unsigned mX, mG, mQ, mS;
  unsigned vX, vG, vQ;
  rsrc_t sX, sG, sU, sQ, sV;
  rsrc_t sP; unsigned vP, pstep;
};

template <int ST> __device__ __forceinline__ void stage_Wa(const Ctx& c, unsigned kG) {
  dma64<GU0 + ST * GU_ST>(c.mG, c.vG, c.sG, kG);
  dma64<GU0 + ST * GU_ST + 1024>(c.mG, c.vG, c.sU, kG);
}
template <int ST> __device__ __forceinline__ void stage_Wb(const Ctx& c, unsigned kG, unsigned kQ) {
  dma48_o128<GU0 + ST * GU_ST + 2048>(c.mG, c.vG, c.sG, kG);
  dma48_o128<GU0 + ST * GU_ST + 2816>(c.mG, c.vG, c.sU, kG);
  dma64<QK0 + ST * QK_ST>(c.mQ, c.vQ, c.sQ, kQ);
  dma32<QK0 + ST * QK_ST + 1024>(c.mQ, c.vQ, c.sV, kQ);
}
template <int ST, int AI> __device__ __forceinline__ void stage_X(const Ctx& c, unsigned kX) {
  dma64<X0 + (ST * 2 + AI) * XH>(c.mX, c.vX, c.sX, kX + AI * 262144u);
  dma64<X0 + (ST * 2 + AI) * XH + 8 * XP>(c.mX, c.vX, c.sX, kX + AI * 262144u + 131072u);
}
template <int ST, int AI> __device__ __forceinline__ void read_X(const Ctx& c, bf16x8 (&xf)[4][2]) {
#pragma unroll
  for (int mt = 0; mt < 4; ++mt)
#pragma unroll
    for (int kk = 0; kk < 2; ++kk) xf[mt][kk] = lds_b128(c.xr + (ST * 2 + AI) * XH + mt * 2 * XP + kk * 64);
}
template <int ST> __device__ __forceinline__ void read_Wa(const Ctx& c, bf16x8 (&wa)[2][2]) {
#pragma unroll
  for (int kk = 0; kk < 2; ++kk) {
    wa[0][kk] = lds_tr2(c.gr + ST * GU_ST + kk * 4 * GU_RB);
    wa[1][kk] = lds_tr2(c.gr + ST * GU_ST + kk * 4 * GU_RB + 1024);
  }
}
template <int ST, bool W3> __device__ __forceinline__ void read_Wb(const Ctx& c, bf16x8 (&wb)[3][2]) {
#pragma unroll
  for (int kk = 0; kk < 2; ++kk) {
    if constexpr (!W3) {
      wb[0][kk] = lds_tr2(c.gr + ST * GU_ST + kk * 4 * GU_RB + 2048);
      wb[1][kk] = lds_tr2(c.gr + ST * GU_ST + kk * 4 * GU_RB + 2816);
      wb[2][kk] = lds_tr2(c.qr + ST * QK_ST + kk * 4 * QK_RB);
    } else {
      wb[0][kk] = lds_tr2(c.qr + ST * QK_ST + kk * 4 * QK_RB);
      wb[1][kk] = lds_tr2(c.qr + ST * QK_ST + kk * 4 * QK_RB + 256);
      wb[2][kk] = lds_tr2(c.qr + ST * QK_ST + kk * 4 * QK_RB + 512);
    }
  }
}
#define PIN_X(xf) asm volatile("" : "+v"(xf[0][0]), "+v"(xf[0][1]), "+v"(xf[1][0]), "+v"(xf[1][1]), "+v"(xf[2][0]), "+v"(xf[2][1]), "+v"(xf[3][0]), "+v"(xf[3][1]))
__device__ __forceinline__ void mma_a(f32x4 (&acc)[4][5], bf16x8 (&xf)[4][2], bf16x8 (&wa)[2][2]) {
  PIN_X(xf);
  __builtin_amdgcn_s_setprio(1);
#pragma unroll
  for (int kk = 0; kk < 2; ++kk)
#pragma unroll
    for (int mt = 0; mt < 4; ++mt)
#pragma unroll
      for (int s = 0; s < 2; ++s) acc[mt][s] = MFMA16(wa[s][kk], xf[mt][kk], acc[mt][s]);
  __builtin_amdgcn_s_setprio(0);
  asm volatile("" : "+v"(acc[0][0]), "+v"(acc[0][1]), "+v"(acc[1][0]), "+v"(acc[1][1]), "+v"(acc[2][0]), "+v"(acc[2][1]), "+v"(acc[3][0]), "+v"(acc[3][1]));
}
template <bool W3> __device__ __forceinline__ void mma_b(f32x4 (&acc)[4][5], bf16x8 (&xf)[4][2], bf16x8 (&wb)[3][2]) {
  PIN_X(xf);
  __builtin_amdgcn_s_setprio(1);
#pragma unroll
  for (int kk = 0; kk < 2; ++kk)
#pragma unroll
    for (int mt = 0; mt < 4; ++mt)
#pragma unroll
      for (int s = 0; s < 3; ++s) {
        if (W3 && s > 0) acc[mt][2 + s] = MFMA16(xf[mt][kk], wb[s][kk], acc[mt][2 + s]);
        else             acc[mt][2 + s] = MFMA16(wb[s][kk], xf[mt][kk], acc[mt][2 + s]);
      }
  __builtin_amdgcn_s_setprio(0);
  asm volatile("" : "+v"(acc[0][2]), "+v"(acc[0][3]), "+v"(acc[0][4]), "+v"(acc[1][2]), "+v"(acc[1][3]), "+v"(acc[1][4]),
                    "+v"(acc[2][2]), "+v"(acc[2][3]), "+v"(acc[2][4]), "+v"(acc[3][2]), "+v"(acc[3][3]), "+v"(acc[3][4]));
}

template <int ST, bool W3>
__device__ __forceinline__ void ktile(const Ctx& c, f32x4 (&acc)[2][4][5], bf16x8 (&xf)[4][2], bf16x8 (&wa)[2][2], bf16x8 (&wb)[3][2],
                                      unsigned kX, unsigned kG, unsigned kQ) {
  read_X<ST, 0>(c, xf); stage_Wa<ST>(c, kG);
  WAIT_VL(14); SCHED(); BAR(); SCHED();
  mma_a(acc[0], xf, wa);
  SCHED(); BAR(); SCHED();
  read_Wb<ST, W3>(c, wb); stage_X<ST, 0>(c, kX);
  WAIT_VL(14); SCHED(); BAR(); SCHED();
  mma_b<W3>(acc[0], xf, wb);
  SCHED(); BAR(); SCHED();
  read_X<ST, 1>(c, xf); stage_Wb<ST>(c, kG, kQ);
  WAIT_VL(16); SCHED(); BAR(); SCHED();
  mma_a(acc[1], xf, wa);
  SCHED(); BAR(); SCHED();
  read_Wa<ST ^ 1>(c, wa); stage_X<ST, 1>(c, kX);
  WAIT_VL(16); SCHED(); BAR(); SCHED();
  mma_b<W3>(acc[1], xf, wb);
  SCHED(); BAR(); SCHED();
}

template <bool W3>
__device__ __forceinline__ void body(const Ctx& c, int wr, int wc, int pm, int j, char* ws, const u16* wd, const u16* wo) {
  f32x4 acc[2][4][5];
#pragma unroll
  for (int a = 0; a < 2; ++a)
#pragma unroll
    for (int m = 0; m < 4; ++m)
#pragma unroll
      for (int s = 0; s < 5; ++s) acc[a][m][s] = (f32x4){0.f, 0.f, 0.f, 0.f};
  bf16x8 xf[4][2], wa[2][2], wb[3][2];
  STAMP_DECL; STAMP_AT(0);
  stage_Wa<0>(c, 0); stage_X<0, 0>(c, 0); stage_Wb<0>(c, 0, 0); stage_X<0, 1>(c, 0);
  stage_Wa<1>(c, 64u * 7168u); stage_X<1, 0>(c, 128); stage_Wb<1>(c, 64u * 7168u, 64u * 6144u); stage_X<1, 1>(c, 128);
  WAIT_V(16); BAR();
  if (wr == 1) BAR();
  read_Wa<0>(c, wa);
  WAIT_L0(); SCHED(); BAR(); SCHED(); BAR(); SCHED();
  STAMP_AT(1);
#pragma unroll 1
  for (int t = 0; t < 16; t += 2) {
    if (t == 2) STAMP_AT(2);
    {
      const int sl = (t >> 1) + PDI;
      if (sl < 8) {
        touch2(c.mS, c.vP, c.sP, (unsigned)sl * c.pstep);
        if (t == 0) {
#pragma unroll
          for (int e = 2; e < PDI; ++e) touch2(c.mS, c.vP, c.sP, (unsigned)e * c.pstep);
        }
      } else {
        int tz_ = threadIdx.x; asm volatile("" : "+v"(tz_));
        const unsigned ln_ = (unsigned)tz_ & 63u;
        const unsigned gl = (ln_ * 2048u + blockIdx.x * 8u + ((unsigned)tz_ >> 6)) * 128u;
        constexpr unsigned PER = (28u + PDI - 1) / PDI;
        touch2(c.mS, ln_ < PER ? gl : 0x80000000u, mk_rsrc(wd, F_ * D_ * 2), (unsigned)(sl - 8) * PER * 2048u * 128u);
        if (sl == 8) touch2(c.mS, gl, mk_rsrc(wo, D_ * D_ * 2), 0u);
      }
    }
    const bool more = t < 14;
    unsigned kX = more ? (unsigned)(t + 2) * 128u : OOB;
    unsigned kG = more ? (unsigned)(t + 2) * 64u * 7168u : OOB;
    unsigned kQ = more ? (unsigned)(t + 2) * 64u * 6144u : OOB;
    ktile<0, W3>(c, acc, xf, wa, wb, kX, kG, kQ);
    ktile<1, W3>(c, acc, xf, wa, wb, kX + 128u, kG + 64u * 7168u, kQ + 64u * 6144u);
  }
  if (wr == 0) BAR();
  STAMP_AT(3);
  int tz = threadIdx.x; asm volatile("" : "+v"(tz));
  const int lane = tz & 63, fr = lane & 15, fq = lane >> 4;
  const int row0 = pm * 256 + wr * 64 + fr;
  char* a2 = ws + WS_A2;
  {
    char* p0 = a2 + ((size_t)(j * 7 + wc) * 2048 + row0) * 32 + fq * 8;
#pragma unroll
    for (int a = 0; a < 2; ++a)
#pragma unroll
      for (int m = 0; m < 4; ++m) {
        const f32x4 g = acc[a][m][0], u = acc[a][m][1];
        u32x2 w; w.x = pk_bf16(geglu(g[0], u[0]), geglu(g[1], u[1])); w.y = pk_bf16(geglu(g[2], u[2]), geglu(g[3], u[3]));
        *(u32x2*)(p0 + (a * 128 + m * 16) * 32) = w;
        if constexpr (!W3) {
          const f32x4 g2 = acc[a][m][2], u2 = acc[a][m][3];
          u32x2 w2; w2.x = pk_bf16(geglu(g2[0], u2[0]), geglu(g2[1], u2[1])); w2.y = pk_bf16(geglu(g2[2], u2[2]), geglu(g2[3], u2[3]));
          *(u32x2*)(p0 + (size_t)4 * 2048 * 32 + (a * 128 + m * 16) * 32) = w2;
        }
      }
  }
  const int qk_off = (j * 64 + pm * 8 + wr * 2) * 2048 + (fq >> 1) * 512 + fr * 16 + (fq & 1) * 8;
  if constexpr (!W3) {
    char* p = ws + (wc == 2 ? WS_K : WS_Q) + qk_off + (wc & 1) * 1024;
#pragma unroll
    for (int a = 0; a < 2; ++a)
#pragma unroll
      for (int m = 0; m < 4; ++m) {
        const f32x4 v = acc[a][m][4];
        u32x2 w; w.x = pk_bf16(v[0], v[1]); w.y = pk_bf16(v[2], v[3]);
        *(u32x2*)(p + (a * 4 + (m >> 1)) * 2048 + (m & 1) * 256) = w;
      }
  } else {
    char* pk = ws + WS_K + qk_off + 1024;
    const int kg0 = pm * 16 + wr * 4;
    char* pv = ws + WS_VT + (size_t)(j * 128 + kg0) * 1024 + (fq & 1) * 512 + fr * 16 + (fq >> 1) * 8;
#pragma unroll
    for (int a = 0; a < 2; ++a)
#pragma unroll
      for (int m = 0; m < 4; ++m) {
        const f32x4 v = acc[a][m][2], v0 = acc[a][m][3], v1 = acc[a][m][4];
        u32x2 w; w.x = pk_bf16(v[0], v[1]); w.y = pk_bf16(v[2], v[3]);
        *(u32x2*)(pk + (a * 4 + (m >> 1)) * 2048 + (m & 1) * 256) = w;
        u32x2 w0; w0.x = pk_bf16(v0[0], v0[1]); w0.y = pk_bf16(v0[2], v0[3]);
        u32x2 w1; w1.x = pk_bf16(v1[0], v1[1]); w1.y = pk_bf16(v1[2], v1[3]);
        *(u32x2*)(pv + (a * 8 + m) * 1024) = w0;
        *(u32x2*)(pv + (a * 8 + m) * 1024 + 256) = w1;
      }
  }
  STAMP_AT(4);
  STAMP_AT(5);
  STAMP_FLUSH(36u << 20, 6);
}
}

__global__ __launch_bounds__(512, 2) void k1_gemm(const u16* __restrict__ x, const u16* __restrict__ wqkv, const u16* __restrict__ wg,
                                                  const u16* __restrict__ wu, const u16* __restrict__ gw, char* __restrict__ ws,
                                                  const u16* __restrict__ wd, const u16* __restrict__ wo) {
  using namespace k1;
  extern __shared__ __attribute__((aligned(16))) char smem[];
  LAS char* lds = (LAS char*)smem;
  const unsigned lbase = (unsigned)(size_t)lds;
  const int tid = threadIdx.x, lane = tid & 63;
  const int wid = __builtin_amdgcn_readfirstlane(tid >> 6), wr = wid >> 2, wc = wid & 3;
  const int bid = blockIdx.x, xcd = bid & 7, idx = bid >> 3;
  const int j = xcd * 4 + (idx >> 3), pm = idx & 7;
  Ctx c;
  c.sX = mk_rsrc(x, S_ * D_ * 2);
  c.sG = mk_rsrc(wg, D_ * F_ * 2);
  c.sU = mk_rsrc(wu, D_ * F_ * 2);
  c.sQ = mk_rsrc(wqkv, D_ * 3072 * 2);
  c.sV = mk_rsrc((const char*)wqkv + 4096, D_ * 3072 * 2 - 4096);
  {
    const int r = lane >> 3, cp = lane & 7, ch = (cp & 4) | ((cp & 3) ^ ((r >> 1) & 3));
    c.vX = (unsigned)((pm * 256 + wid * 8 + r) * 2048 + ch * 16);
    const int u = lane >> 4, rr = (lane >> 1) & 7, hf = lane & 1;
    c.vG = (unsigned)((wid * 8 + rr) * 7168 + j * 224 + u * 32 + hf * 16);
    c.vQ = (unsigned)((wid * 8 + rr) * 6144 + (u >> 1) * 2048 + j * 64 + (u & 1) * 32 + hf * 16);
    c.mS = lbase + SCR; c.mX = lbase + wid * XP; c.mG = lbase + wid * GU_RB; c.mQ = lbase + wid * QK_RB;
    const int fr = lane & 15, q = lane >> 4;
    c.xr = lds + X0 + (wr * 8 + (fr >> 3)) * XP + (fr & 7) * 128 + ((q ^ ((fr >> 1) & 3)) * 16);
    const int qq = (lane & 15) >> 2, p = lane & 3;
    c.gr = lds + GU0 + q * GU_RB + qq * 32 + p * 8 + wc * 256;
    c.qr = lds + QK0 + q * QK_RB + qq * 32 + p * 8 + wc * 256;
  }
  {
    const unsigned i = (unsigned)(lane * 64 + idx * 2 + wr);
    if (wc < 2) { c.sP = wc ? c.sU : c.sG; c.pstep = 128u * 7168u; c.vP = i < 896u ? (i / 7u) * 7168u + xcd * 896u + (i % 7u) * 128u : 0x80000000u; }
    else if (wc == 2) { c.sP = c.sQ; c.pstep = 128u * 6144u; const unsigned e = i % 6u; c.vP = i < 768u ? (i / 6u) * 6144u + (e >> 1) * 2048u + xcd * 256u + (e & 1u) * 128u : 0x80000000u; }
    else { c.sP = c.sX; c.pstep = 256u; c.vP = (i >> 1) * 2048u + (i & 1u) * 128u; }
  }
  if (wc == 3) body<true>(c, wr, wc, pm, j, ws, wd, wo); else body<false>(c, wr, wc, pm, j, ws, wd, wo);
  {
    const int row = pm * 256 + j * 8 + wid;
    const u32x4* xp = (const u32x4*)(x + (size_t)row * D_) + lane * 2;
    const u32x4* gp = (const u32x4*)gw + lane * 2;
    float s = 0.f;
#pragma unroll
    for (int i = 0; i < 2; ++i) {
      const u32x4 a = xp[i], b = gp[i];
#pragma unroll
      for (int e = 0; e < 4; ++e) s += bf_lo(a[e]) * bf_lo(b[e]) + bf_hi(a[e]) * bf_hi(b[e]);
    }
#pragma unroll
    for (int o = 32; o > 0; o >>= 1) s += __shfl_xor(s, o);
    if (lane == 0) ((float*)(ws + WS_G))[row] = 1.0f / (1.0f + __expf(-s));
    if (bid == 0 && tid < 136) ((unsigned*)(ws + WS_FLAG))[tid * 64] = 0u;
  }
}

#define KX_TILES 16
namespace k2 {
constexpr int B_RB = 1024 + 32, B_H = 8 * B_RB, ST_A = 16384, ST_B = 2 * B_H, ST = ST_A + ST_B, NST = 4, LDS_BYTES = NST * ST;
constexpr int NT_DOWN = 56, NT_WO = 16, KX = KX_TILES;
constexpr unsigned OOB = 0x40000000u;
__device__ __forceinline__ bf16x8 lds_tr2b(LAS char* p) {
  s16x4 a = __builtin_amdgcn_ds_read_tr16_b64_v4i16((LAS s16x4*)p);
  s16x4 b = __builtin_amdgcn_ds_read_tr16_b64_v4i16((LAS s16x4*)(p + 512));
  s16x8 r = __builtin_shufflevector(a, b, 0, 1, 2, 3, 4, 5, 6, 7);
  return __builtin_bit_cast(bf16x8, r);
}
__device__ __forceinline__ void wait_flag(const unsigned* fl, unsigned need) {
  unsigned spins = 0;
  while ((unsigned)__builtin_amdgcn_readfirstlane(__hip_atomic_load(fl, __ATOMIC_RELAXED, __HIP_MEMORY_SCOPE_AGENT)) < need) {
    __builtin_amdgcn_s_sleep(2);
    if (++spins > (1u << 22)) break;
  }
  __builtin_amdgcn_fence(__ATOMIC_ACQUIRE, "agent");
  asm volatile("s_waitcnt vmcnt(0)" ::: "memory");
}
template <int I0, int NT>
__device__ __forceinline__ void attn_chunk(LAS char* kl, LAS char* vl, bf16x8 qf0, bf16x8 qf1, int qt, int l31, int hh, float& mrun, f32x2& lrun, f32x16& oacc) {
  const float NEG = -__builtin_inff();
  const float cs = 0.17677669529663687f * 1.4426950408889634f;
  f32x16 sc[NT];
#pragma unroll
  for (int i = 0; i < NT; ++i) {
    f32x16 z = {};
    z = MFMA32(lds_b128(kl + (I0 + i) * 2048), qf0, z);
    sc[i] = MFMA32(lds_b128(kl + (I0 + i) * 2048 + 1024), qf1, z);
  }
#pragma unroll
  for (int r = 0; r < 16; ++r) {
    const int kl_ = (r & 3) + 8 * (r >> 2) + 4 * hh;
    if constexpr (I0 == 0) sc[0][r] = (kl_ > l31) ? sc[0][r] : NEG;
    if constexpr (I0 + NT == 9) sc[NT - 1][r] = (kl_ <= l31) ? sc[NT - 1][r] : NEG;
  }
  float mx = mrun;
#pragma unroll
  for (int i = 0; i < NT; ++i) {
    float ma = fmaxf(sc[i][0], sc[i][1]), mb = fmaxf(sc[i][2], sc[i][3]);
#pragma unroll
    for (int r = 4; r < 16; r += 4) { ma = fmaxf(fmaxf(ma, sc[i][r]), sc[i][r + 1]); mb = fmaxf(fmaxf(mb, sc[i][r + 2]), sc[i][r + 3]); }
    const float bias = (qt - 8 + I0 + i < 0) ? NEG : 0.f;
    mx = fmaxf(mx, fmaxf(ma, mb) + bias);
  }
  mx = fmaxf(mx, __shfl_xor(mx, 32));
  const float mxs = fmaxf(mx, -3.0e38f);
  if constexpr (I0 != 0) {
    const float al = __builtin_amdgcn_exp2f((mrun - mxs) * cs);
    lrun *= al;
#pragma unroll
    for (int r = 0; r < 16; ++r) oacc[r] *= al;
  }
  mrun = mx;
  const f32x2 cs2 = {cs, cs};
  const float mc = -mxs * cs;
  f32x2 l2[2] = {lrun, {0.f, 0.f}};
#pragma unroll
  for (int i = 0; i < NT; ++i) {
    const float mci = mc + ((qt - 8 + I0 + i < 0) ? NEG : 0.f);
    const f32x2 mc2 = {mci, mci};
#pragma unroll
    for (int s = 0; s < 2; ++s) {
      u32x4 pw;
#pragma unroll
      for (int e = 0; e < 4; ++e) {
        const f32x2 sv = {sc[i][8 * s + 2 * e], sc[i][8 * s + 2 * e + 1]};
        const f32x2 t = __builtin_elementwise_fma(sv, cs2, mc2);
        f32x2 p; p.x = __builtin_amdgcn_exp2f(t.x); p.y = __builtin_amdgcn_exp2f(t.y);
        l2[e & 1] += p;
        pw[e] = pk_bf16(p.x, p.y);
      }
      oacc = MFMA32(lds_b128(vl + (I0 + i) * 2048 + s * 1024), __builtin_bit_cast(bf16x8, pw), oacc);
    }
  }
  lrun = l2[0] + l2[1];
}
__device__ __forceinline__ void attn_head(LAS char* kl, LAS char* vl, bf16x8 qf0, bf16x8 qf1, int qt, int lane, rsrc_t sA, int head, float gq) {
  const int l31 = lane & 31, hh = lane >> 5;
  float mrun = -__builtin_inff(); f32x2 lrun = {0.f, 0.f}; f32x16 oacc = {};
  attn_chunk<0, 5>(kl, vl, qf0, qf1, qt, l31, hh, mrun, lrun, oacc);
  asm volatile("" : "+v"(oacc), "+v"(lrun), "+v"(mrun), "+v"(qf0), "+v"(qf1)); SCHED();
  attn_chunk<5, 4>(kl, vl, qf0, qf1, qt, l31, hh, mrun, lrun, oacc);
  float sum = lrun.x + lrun.y;
  sum += __shfl_xor(sum, 32);
  const float inv = gq / sum;
#pragma unroll
  for (int g2 = 0; g2 < 2; ++g2) {
    unsigned a0 = pk_bf16(oacc[8 * g2 + 0] * inv, oacc[8 * g2 + 1] * inv), a1 = pk_bf16(oacc[8 * g2 + 2] * inv, oacc[8 * g2 + 3] * inv);
    unsigned b0 = pk_bf16(oacc[8 * g2 + 4] * inv, oacc[8 * g2 + 5] * inv), b1 = pk_bf16(oacc[8 * g2 + 6] * inv, oacc[8 * g2 + 7] * inv);
    auto r0 = __builtin_amdgcn_permlane32_swap(a0, b0, false, false);
    auto r1 = __builtin_amdgcn_permlane32_swap(a1, b1, false, false);
    u32x4 w; w.x = r0[0]; w.y = r1[0]; w.z = r0[1]; w.w = r1[1];
    __builtin_amdgcn_raw_buffer_store_b128(w, sA, (unsigned)(((224 + head * 2 + g2) * 2048 + qt * 32 + l31) * 32 + hh * 16), 0, 16);
  }
}

template <int MC> __device__ __forceinline__ void dma4k(unsigned mbase, unsigned voff, rsrc_t srd, unsigned soff) {
  asm volatile("s_add_u32 m0, %0, %4\n\ts_nop 0\n\tbuffer_load_dwordx4 %1, %2, %3 offen lds\n\tbuffer_load_dwordx4 %1, %2, %3 offen offset:1024 lds\n\t"
               "buffer_load_dwordx4 %1, %2, %3 offen offset:2048 lds\n\tbuffer_load_dwordx4 %1, %2, %3 offen offset:3072 lds"
               :: "s"(mbase), "v"(voff), "s"(srd), "s"(soff), "i"(MC) : "memory", "scc");
}
struct LCtx { unsigned vA, vB0, vB1, mA, mB; rsrc_t sA; };
template <int SL> __device__ __forceinline__ void stage(const LCtx& c, rsrc_t sB, unsigned ga, unsigned kb) {
  dma4k<SL * ST>(c.mA, c.vA, c.sA, ga);
  dma64<SL * ST>(c.mB, c.vB0, sB, kb); dma64_o128<SL * ST + B_H>(c.mB, c.vB0, sB, kb);
  dma64<SL * ST + B_RB>(c.mB, c.vB1, sB, kb); dma64_o128<SL * ST + B_RB + B_H>(c.mB, c.vB1, sB, kb);
}
struct Ctx { LAS char* ar; LAS char* br0; LAS char* br1; LAS char* ar2; LAS char* br02; LAS char* br12; };
template <int SL, int M>
__device__ __forceinline__ void quarter(const Ctx& c, f32x4 (&acc)[4][4], bf16x8 (&af)[4], bf16x8 (&bf)[4], bf16x8 (&an)[4], bf16x8 (&bn)[4]) {
  constexpr int ns = (SL + 1) & 3, so = (ns & 1) * ST;
  LAS char* const ar = (ns & 2) ? c.ar2 : c.ar; LAS char* const br0 = (ns & 2) ? c.br02 : c.br0; LAS char* const br1 = (ns & 2) ? c.br12 : c.br1;
  asm volatile("" : "+v"(af[M]));
#pragma unroll
  for (int n = 0; n < 4; ++n) acc[M][n] = MFMA16(bf[n], af[M], acc[M][n]);
  asm volatile("" : "+v"(acc[M][0]), "+v"(acc[M][1]), "+v"(acc[M][2]), "+v"(acc[M][3]));
  SCHED();
  if constexpr (M < 2) {
    bn[2 * M] = lds_tr2b((M ? br1 : br0) + so); bn[2 * M + 1] = lds_tr2b((M ? br1 : br0) + so + 32);
    an[2 * M] = lds_b128(ar + so + 2 * M * 512);
  }
  if constexpr (M == 2) { an[1] = lds_b128(ar + so + 512); an[3] = lds_b128(ar + so + 3 * 512); }
  SCHED();
}
template <int SL>
__device__ __forceinline__ void step(const Ctx& c, f32x4 (&acc)[4][4], bf16x8 (&af)[4], bf16x8 (&bf)[4], bf16x8 (&an)[4], bf16x8 (&bn)[4]) {
  quarter<SL, 0>(c, acc, af, bf, an, bn); quarter<SL, 1>(c, acc, af, bf, an, bn);
  quarter<SL, 2>(c, acc, af, bf, an, bn); quarter<SL, 3>(c, acc, af, bf, an, bn);
  WAIT_L0(); SCHED(); BAR(); SCHED();
}
}

__global__ __launch_bounds__(768, 3) void k2_gemm(const u16* __restrict__ wo, const u16* __restrict__ wd, char* __restrict__ ws, u16* __restrict__ out) {
  using namespace k2;
  extern __shared__ __attribute__((aligned(16))) char smem[];
  LAS char* lds = (LAS char*)smem;
  const unsigned lbase = (unsigned)(size_t)lds;
  const int tid = threadIdx.x, lane = tid & 63;
  const int wid = __builtin_amdgcn_readfirstlane(tid >> 6);
  const int bid = blockIdx.x, xcd = bid & 7, idx = bid >> 3;
  const bool roleX = (idx & 1) != 0;
  const int mt = (xcd >> 1) * 4 + (idx >> 3), nt = (xcd & 1) * 4 + ((idx >> 1) & 3);
  const int panel = mt >> 1, tile = mt * 8 + nt;
  unsigned* flags = (unsigned*)(ws + WS_FLAG);
  const int nq = roleX ? KX / 4 : (NT_DOWN - KX + NT_WO) / 4, q_sw = roleX ? 1000 : (NT_DOWN - KX) / 4 - 1;
  const int head0 = 2 * ((mt & 1) * 8 + nt);

  if (wid >= 8) {
    const int l = wid - 8;
    rsrc_t sB = mk_rsrc(wd, roleX ? KX * 131072 : F_ * D_ * 2);
    const rsrc_t sO = mk_rsrc(wo, D_ * D_ * 2);
    LCtx c;
    c.sA = mk_rsrc(ws + WS_A2, roleX ? KX * 262144 : A2_BYTES);
    c.vA = (unsigned)((l * 2048 + mt * 128) * 32 + lane * 16);
    c.vB0 = (unsigned)((l * 16 + (lane >> 3)) * 2048 + nt * 256 + (((lane & 7) ^ ((lane >> 2) & 4)) * 16)); c.vB1 = c.vB0 + 8u * 2048u;
    c.mA = lbase + l * 4096; c.mB = lbase + ST_A + l * 2 * B_RB;
    unsigned ga = roleX ? 0u : KX * 262144u, kb = roleX ? 0u : KX * 131072u;
    if (roleX) {
      const rsrc_t sK = mk_rsrc(ws + WS_K, 4u << 20), sV = mk_rsrc(ws + WS_VT, 4u << 20);
      const unsigned so = (panel == 0 && l < 2) ? OOB : (unsigned)(head0 * 131072 + panel * 16384 - 16384 + l * 8192);
      const unsigned vl = lane * 16, mk = lbase + l * 8192;
      dma4k<0>(mk, vl, sK, so); dma4k<4096>(mk, vl, sK, so + 4096); dma4k<32768>(mk, vl, sV, so); dma4k<32768 + 4096>(mk, vl, sV, so + 4096);
      const unsigned s1 = so + 131072;
      dma4k<2 * ST>(mk, vl, sK, s1); dma4k<2 * ST + 4096>(mk, vl, sK, s1 + 4096); dma4k<2 * ST + 32768>(mk, vl, sV, s1); dma4k<2 * ST + 32768 + 4096>(mk, vl, sV, s1 + 4096);
      WAIT_V(16); BAR();
      WAIT_V(0); BAR();
      stage<0>(c, sB, ga, kb); stage<1>(c, sB, ga + 262144u, kb + 131072u);
      BAR();
    } else {
      stage<0>(c, sB, ga, kb); stage<1>(c, sB, ga + 262144u, kb + 131072u);
    }
    stage<2>(c, sB, ga + 2u * 262144u, kb + 2u * 131072u); stage<3>(c, sB, ga + 3u * 262144u, kb + 3u * 131072u);
    ga += 4u * 262144u; kb += 4u * 131072u;
    WAIT_V(16); BAR();
    BAR();
#pragma unroll 1
    for (int q = 0; q < nq; ++q) {
      if (q == q_sw - 1 && l == 0) wait_flag(flags + panel * 64, 16u);
      if (q == q_sw) { sB = sO; kb = 0; }
      stage<0>(c, sB, ga, kb); WAIT_V(16); BAR();
      stage<1>(c, sB, ga + 262144u, kb + 131072u); WAIT_V(16); BAR();
      stage<2>(c, sB, ga + 2u * 262144u, kb + 2u * 131072u); WAIT_V(16); BAR();
      stage<3>(c, sB, ga + 3u * 262144u, kb + 3u * 131072u); WAIT_V(16); BAR();
      ga += 4u * 262144u; kb += 4u * 131072u;
    }
    WAIT_V(0);
    if (!roleX && l == 0) wait_flag(flags + (8 + tile) * 64, 1u);
    BAR();
    return;
  }

  const int kg = wid >> 2, wm = (wid >> 1) & 1, wn = wid & 1;
  const int fr = lane & 15, fq = lane >> 4;
  STAMP_DECL; STAMP_AT(0);
  if (roleX) {
    const int qt = panel * 8 + wid;
    const rsrc_t sA = mk_rsrc(ws + WS_A2, A2_BYTES);
    const char* qp = ws + WS_Q + (size_t)(head0 * 64 + qt) * 2048 + lane * 16;
    const bf16x8 qa0 = *(const bf16x8*)qp, qa1 = *(const bf16x8*)(qp + 1024);
    const float gq = ((const float*)(ws + WS_G))[qt * 32 + (lane & 31)];
    BAR();
    STAMP_AT(1);
    attn_head(lds + wid * 2048 + lane * 16, lds + 32768 + wid * 2048 + lane * 16, qa0, qa1, qt, lane, sA, head0, gq);
    const bf16x8 qb0 = *(const bf16x8*)(qp + 131072), qb1 = *(const bf16x8*)(qp + 131072 + 1024);
    WAIT_L0(); BAR();
    attn_head(lds + 2 * ST + wid * 2048 + lane * 16, lds + 2 * ST + 32768 + wid * 2048 + lane * 16, qb0, qb1, qt, lane, sA, head0 + 1, gq);
    WAIT_VL(0); BAR();
    if (tid == 0) __hip_atomic_fetch_add(flags + panel * 64, 1u, __ATOMIC_RELAXED, __HIP_MEMORY_SCOPE_AGENT);
  } else {
    STAMP_AT(1);
  }
  STAMP_AT(2);
  Ctx c;
  c.ar = lds + (kg * 2 + (fq >> 1)) * 4096 + (wm * 64 + fr) * 32 + (fq & 1) * 16;
  c.br0 = lds + ST_A + wn * B_H + (kg * 4 + fq) * B_RB + (fr >> 2) * 128 + ((fr >> 3) * 64) + (lane & 3) * 8;
  c.br1 = lds + ST_A + wn * B_H + (kg * 4 + fq) * B_RB + (fr >> 2) * 128 + (((fr >> 3) ^ 1) * 64) + (lane & 3) * 8;
  c.ar2 = c.ar + 2 * ST; c.br02 = c.br0 + 2 * ST; c.br12 = c.br1 + 2 * ST;
  asm volatile("" : "+v"(c.ar2), "+v"(c.br02), "+v"(c.br12));
  float og[4];
  { const float* gptr = (const float*)(ws + WS_G) + mt * 128 + wm * 64 + fr;
#pragma unroll
    for (int m = 0; m < 4; ++m) og[m] = 1.0f - gptr[m * 16];
    asm volatile("" : "+v"(og[0]), "+v"(og[1]), "+v"(og[2]), "+v"(og[3])); }
  f32x4 acc[4][4];
#pragma unroll
  for (int m = 0; m < 4; ++m)
#pragma unroll
    for (int n = 0; n < 4; ++n) acc[m][n] = (f32x4){0.f, 0.f, 0.f, 0.f};
  BAR();
  STAMP_AT(3);
  {
    bf16x8 af0[4], bf0[4], af1[4], bf1[4];
    bf0[0] = lds_tr2b(c.br0); bf0[1] = lds_tr2b(c.br0 + 32); bf0[2] = lds_tr2b(c.br1); bf0[3] = lds_tr2b(c.br1 + 32);
#pragma unroll
    for (int m = 0; m < 4; ++m) af0[m] = lds_b128(c.ar + m * 512);
    WAIT_L0(); SCHED(); BAR(); SCHED();
#pragma unroll 1
    for (int q = 0; q < nq; ++q) {
      if (q == q_sw + 1) {
#pragma unroll
        for (int m = 0; m < 4; ++m)
#pragma unroll
          for (int n = 0; n < 4; ++n) acc[m][n] *= og[m];
      }
      step<0>(c, acc, af0, bf0, af1, bf1);
      step<1>(c, acc, af1, bf1, af0, bf0);
      step<2>(c, acc, af0, bf0, af1, bf1);
      step<3>(c, acc, af1, bf1, af0, bf0);
    }
  }
  STAMP_AT(4);
  BAR();
  STAMP_AT(5);
  int tz = threadIdx.x; asm volatile("" : "+v"(tz));
  const int ln = tz & 63, fr2 = ln & 15, fq2 = ln >> 4;
  const float gv[2] = {kg ? og[2] : og[0], kg ? og[3] : og[1]};
  const rsrc_t sP = mk_rsrc(ws + WS_PART + (size_t)tile * 65536, 65536);
  f32x4 pp[2][4];
  if (!roleX) {
#pragma unroll
    for (int m = 0; m < 2; ++m)
#pragma unroll
      for (int n = 0; n < 4; ++n)
        pp[m][n] = __builtin_bit_cast(f32x4, __builtin_amdgcn_raw_buffer_load_b128(sP, (unsigned)(ln * 16), (unsigned)((wid * 8 + m * 4 + n) * 1024), 0));
  }
  LAS f32x4* red = (LAS f32x4*)lds + ln;
  f32x4 r[2][4];
  if (kg == 0) {
#pragma unroll
    for (int m = 0; m < 2; ++m)
#pragma unroll
      for (int n = 0; n < 4; ++n) { red[(wid * 8 + m * 4 + n) * 64] = acc[2 + m][n]; r[m][n] = acc[m][n]; }
  } else {
#pragma unroll
    for (int m = 0; m < 2; ++m)
#pragma unroll
      for (int n = 0; n < 4; ++n) { red[(wid * 8 + m * 4 + n) * 64] = acc[m][n]; r[m][n] = acc[2 + m][n]; }
  }
  WAIT_L0(); BAR();
#pragma unroll
  for (int m = 0; m < 2; ++m)
#pragma unroll
    for (int n = 0; n < 4; ++n) r[m][n] += red[((wid ^ 4) * 8 + m * 4 + n) * 64];
  if (roleX) {
#pragma unroll
    for (int m = 0; m < 2; ++m)
#pragma unroll
      for (int n = 0; n < 4; ++n)
        __builtin_amdgcn_raw_buffer_store_b128(__builtin_bit_cast(u32x4, r[m][n]), sP, (unsigned)(ln * 16), (unsigned)((wid * 8 + m * 4 + n) * 1024), 16);
    asm volatile("s_waitcnt vmcnt(0)" ::: "memory");
    BAR();
    if (tid == 0) __hip_atomic_store(flags + (8 + tile) * 64, 1u, __ATOMIC_RELAXED, __HIP_MEMORY_SCOPE_AGENT);
  } else {
    u16* op = out + (size_t)(mt * 128 + wm * 64 + kg * 32 + fr2) * D_ + nt * 128 + wn * 64 + fq2 * 4;
#pragma unroll
    for (int m = 0; m < 2; ++m) {
#pragma unroll
      for (int n = 0; n < 4; ++n) {
        const f32x4 v = r[m][n] + pp[m][n] * gv[m];
        u32x2 w; w.x = pk_bf16(v[0], v[1]); w.y = pk_bf16(v[2], v[3]);
        *(u32x2*)(op + m * 16 * D_ + n * 16) = w;
      }
    }
  }
  STAMP_AT(6);
  STAMP_FLUSH(37u << 20, 7);
}

extern "C" void kernel_launch(void* const* d_in, const int* in_sizes, int n_in, void* d_out, int out_size, void* d_ws, size_t ws_size,
                              hipStream_t stream) {
  const u16* x = (const u16*)d_in[0]; const u16* wqkv = (const u16*)d_in[1]; const u16* wo = (const u16*)d_in[2];
  const u16* wg = (const u16*)d_in[4]; const u16* wu = (const u16*)d_in[5]; const u16* wd = (const u16*)d_in[6]; const u16* gw = (const u16*)d_in[7];
  (void)hipFuncSetAttribute((const void*)k1_gemm, hipFuncAttributeMaxDynamicSharedMemorySize, k1::LDS_BYTES);
  (void)hipFuncSetAttribute((const void*)k2_gemm, hipFuncAttributeMaxDynamicSharedMemorySize, k2::LDS_BYTES);
  k1_gemm<<<256, 512, k1::LDS_BYTES, stream>>>(x, wqkv, wg, wu, gw, (char*)d_ws, wd, wo);
  k2_gemm<<<256, 768, k2::LDS_BYTES, stream>>>(wo, wd, (char*)d_ws, (u16*)d_out);
}
```

```cpp
#include <hip/hip_runtime.h>
#include <hip/hip_bf16.h>

#define LAS __attribute__((address_space(3)))
typedef unsigned short u16;
typedef short s16x4 __attribute__((ext_vector_type(4)));
typedef short s16x8 __attribute__((ext_vector_type(8)));
typedef __bf16 bf16x8 __attribute__((ext_vector_type(8)));
typedef __bf16 bf16x2 __attribute__((ext_vector_type(2)));
typedef float f32x2 __attribute__((ext_vector_type(2)));
typedef float f32x4 __attribute__((ext_vector_type(4)));
typedef float f32x16 __attribute__((ext_vector_type(16)));
typedef unsigned u32x2 __attribute__((ext_vector_type(2)));
typedef unsigned u32x4 __attribute__((ext_vector_type(4)));
typedef __amdgpu_buffer_rsrc_t rsrc_t;

constexpr int S_ = 2048, D_ = 1024, F_ = 3584;
typedef unsigned long long u64;
constexpr size_t WS_A2 = 0;
constexpr size_t WS_KV = 20u << 20;
constexpr size_t WS_G  = 32u << 20;
constexpr size_t WS_FLAG = 33u << 20;
constexpr size_t WS_PART = 40u << 20;
constexpr unsigned A2_BYTES = 288u * 2048u * 32u;
constexpr unsigned FL_KV = 4096, FL_H = 8192, FL_O = 12288, FL_P = 16384;

#define STAMP_AT(i)
#define BAR() do { asm volatile("" ::: "memory"); __builtin_amdgcn_s_barrier(); asm volatile("" ::: "memory"); } while (0)
#define SCHED() __builtin_amdgcn_sched_barrier(0)
#define WAIT_VL(n) do { asm volatile("s_waitcnt vmcnt(" #n ")" ::: "memory"); __builtin_amdgcn_s_waitcnt(0xC07F); } while (0)
#define WAIT_V(n) asm volatile("s_waitcnt vmcnt(" #n ")" ::: "memory")
#define WAIT_L0() do { asm volatile("" ::: "memory"); __builtin_amdgcn_s_waitcnt(0xC07F); } while (0)

__device__ __forceinline__ rsrc_t mk_rsrc(const void* p, unsigned bytes) {
  return __builtin_amdgcn_make_buffer_rsrc((void*)p, (short)0, (int)bytes, 0x00020000);
}
template <int MC> __device__ __forceinline__ void dma64(unsigned mbase, unsigned voff, rsrc_t srd, unsigned soff) {
  asm volatile("s_add_u32 m0, %0, %4\n\ts_nop 0\n\tbuffer_load_dwordx4 %1, %2, %3 offen lds"
               :: "s"(mbase), "v"(voff), "s"(srd), "s"(soff), "i"(MC) : "memory", "scc");
}
template <int MC> __device__ __forceinline__ void dma64_o128(unsigned mbase, unsigned voff, rsrc_t srd, unsigned soff) {
  asm volatile("s_add_u32 m0, %0, %4\n\ts_nop 0\n\tbuffer_load_dwordx4 %1, %2, %3 offen offset:128 lds"
               :: "s"(mbase), "v"(voff), "s"(srd), "s"(soff), "i"(MC - 128) : "memory", "scc");
}
template <int MC> __device__ __forceinline__ void dma48_o128(unsigned mbase, unsigned voff, rsrc_t srd, unsigned soff) {
  asm volatile("s_add_u32 m0, %0, %4\n\ts_mov_b32 exec_hi, 0xffff\n\tbuffer_load_dwordx4 %1, %2, %3 offen offset:128 lds\n\ts_mov_b32 exec_hi, -1"
               :: "s"(mbase), "v"(voff), "s"(srd), "s"(soff), "i"(MC - 128) : "memory", "scc");
}
template <int MC> __device__ __forceinline__ void dma48(unsigned mbase, unsigned voff, rsrc_t srd, unsigned soff) {
  asm volatile("s_add_u32 m0, %0, %4\n\ts_mov_b32 exec_hi, 0xffff\n\tbuffer_load_dwordx4 %1, %2, %3 offen lds\n\ts_mov_b32 exec_hi, -1"
               :: "s"(mbase), "v"(voff), "s"(srd), "s"(soff), "i"(MC) : "memory", "scc");
}
template <int MC> __device__ __forceinline__ void dma32(unsigned mbase, unsigned voff, rsrc_t srd, unsigned soff) {
  asm volatile("s_add_u32 m0, %0, %4\n\ts_mov_b32 exec_hi, 0\n\tbuffer_load_dwordx4 %1, %2, %3 offen lds\n\ts_mov_b32 exec_hi, -1"
               :: "s"(mbase), "v"(voff), "s"(srd), "s"(soff), "i"(MC) : "memory", "scc");
}
__device__ __forceinline__ void touch(unsigned mscr, unsigned voff, rsrc_t srd) {
  asm volatile("s_mov_b32 m0, %0\n\ts_nop 0\n\tbuffer_load_dword %1, %2, 0 offen lds" :: "s"(mscr), "v"(voff), "s"(srd) : "memory");
}
__device__ __forceinline__ bf16x8 lds_b128(LAS char* p) { return *(LAS bf16x8*)p; }
__device__ __forceinline__ bf16x8 lds_tr2(LAS char* p) {
  s16x4 a = __builtin_amdgcn_ds_read_tr16_b64_v4i16((LAS s16x4*)p);
  s16x4 b = __builtin_amdgcn_ds_read_tr16_b64_v4i16((LAS s16x4*)(p + 128));
  s16x8 r = __builtin_shufflevector(a, b, 0, 1, 2, 3, 4, 5, 6, 7);
  return __builtin_bit_cast(bf16x8, r);
}
__device__ __forceinline__ unsigned pk_bf16(float lo, float hi) {
  bf16x2 v; v.x = (__bf16)lo; v.y = (__bf16)hi; return __builtin_bit_cast(unsigned, v);
}
__device__ __forceinline__ float bf_lo(unsigned u) { return __uint_as_float(u << 16); }
__device__ __forceinline__ float bf_hi(unsigned u) { return __uint_as_float(u & 0xffff0000u); }
#define MFMA16(a, b, c) __builtin_amdgcn_mfma_f32_16x16x32_bf16((a), (b), (c), 0, 0, 0)
#define MFMA32(a, b, c) __builtin_amdgcn_mfma_f32_32x32x16_bf16((a), (b), (c), 0, 0, 0)

__device__ __forceinline__ unsigned geglu2(float g0, float g1, float u0, float u1) {
  const float c1 = -2.0f * 0.7978845608028654f * 1.4426950408889634f;
  const float c2 = c1 * 0.044715f;
  const f32x2 g = {g0, g1}, u = {u0, u1}, k1 = {c1, c1}, k2 = {c2, c2}, one = {1.0f, 1.0f};
  const f32x2 z = g * __builtin_elementwise_fma(g * g, k2, k1);
  f32x2 e; e.x = __builtin_amdgcn_exp2f(z.x); e.y = __builtin_amdgcn_exp2f(z.y);
  const f32x2 d = e + one;
  f32x2 r; r.x = __builtin_amdgcn_rcpf(d.x); r.y = __builtin_amdgcn_rcpf(d.y);
  const f32x2 h = (g * u) * r;
  return pk_bf16(h.x, h.y);
}
__device__ __forceinline__ bool arrive8(LAS unsigned* cnt, int lane) {
  unsigned old = 0;
  if (lane == 0) old = __hip_atomic_fetch_add(cnt, 1u, __ATOMIC_RELAXED, __HIP_MEMORY_SCOPE_WORKGROUP);
  return __builtin_amdgcn_readfirstlane(old) == 7u;
}

namespace k1 {
constexpr int XP = 1088, XH = 16 * XP;
constexpr int X0 = 0;
constexpr int GU_RB = 14 * 256 + 128, GU_ST = 8 * GU_RB, GU0 = 4 * XH;
constexpr int QK_RB = 6 * 256 + 128, QK_ST = 8 * QK_RB, QK0 = GU0 + 2 * GU_ST;
constexpr int SCR = QK0 + 2 * QK_ST, LDS_BYTES = SCR + 256;
constexpr unsigned OOB = 0x40000000u;

struct Ctx {
  LAS char* xr; LAS char* gr; LAS char* qr;
  unsigned mX, mG, mQ, mS, dB;
  unsigned vX, vG, vQ;
  rsrc_t sX, sG, sU, sQ, sV;
};

template <int ST> __device__ __forceinline__ void stage_Wa(const Ctx& c, unsigned kG) {
  dma64<GU0 + ST * GU_ST>(c.mG, c.vG, c.sG, kG);
  dma64<GU0 + ST * GU_ST + 1024>(c.mG, c.vG, c.sU, kG);
}
template <int ST> __device__ __forceinline__ void stage_Wb(const Ctx& c, unsigned kG, unsigned kQ) {
  dma48<GU0 + ST * GU_ST + 2048>(c.mG, c.vG, c.sG, kG + c.dB);
  dma48<GU0 + ST * GU_ST + 2816>(c.mG, c.vG, c.sU, kG + c.dB);
  dma64<QK0 + ST * QK_ST>(c.mQ, c.vQ, c.sQ, kQ);
  dma32<QK0 + ST * QK_ST + 1024>(c.mQ, c.vQ, c.sV, kQ);
}
template <int ST, int AI> __device__ __forceinline__ void stage_X(const Ctx& c, unsigned kX) {
  dma64<X0 + (ST * 2 + AI) * XH>(c.mX, c.vX, c.sX, kX + AI * 262144u);
  dma64<X0 + (ST * 2 + AI) * XH + 8 * XP>(c.mX, c.vX, c.sX, kX + AI * 262144u + 131072u);
}
template <int ST, int AI> __device__ __forceinline__ void read_X(const Ctx& c, bf16x8 (&xf)[4][2]) {
#pragma unroll
  for (int mt = 0; mt < 4; ++mt)
#pragma unroll
    for (int kk = 0; kk < 2; ++kk) xf[mt][kk] = lds_b128(c.xr + (ST * 2 + AI) * XH + mt * 2 * XP + kk * 64);
}
template <int ST> __device__ __forceinline__ void read_Wa(const Ctx& c, bf16x8 (&wa)[2][2]) {
#pragma unroll
  for (int kk = 0; kk < 2; ++kk) {
    wa[0][kk] = lds_tr2(c.gr + ST * GU_ST + kk * 4 * GU_RB);
    wa[1][kk] = lds_tr2(c.gr + ST * GU_ST + kk * 4 * GU_RB + 1024);
  }
}
template <int ST, bool W3> __device__ __forceinline__ void read_Wb(const Ctx& c, bf16x8 (&wb)[3][2]) {
#pragma unroll
  for (int kk = 0; kk < 2; ++kk) {
    if constexpr (!W3) {
      wb[0][kk] = lds_tr2(c.gr + ST * GU_ST + kk * 4 * GU_RB + 2048);
      wb[1][kk] = lds_tr2(c.gr + ST * GU_ST + kk * 4 * GU_RB + 2816);
      wb[2][kk] = lds_tr2(c.qr + ST * QK_ST + kk * 4 * QK_RB);
    } else {
      wb[0][kk] = lds_tr2(c.qr + ST * QK_ST + kk * 4 * QK_RB);
      wb[1][kk] = lds_tr2(c.qr + ST * QK_ST + kk * 4 * QK_RB + 256);
      wb[2][kk] = lds_tr2(c.qr + ST * QK_ST + kk * 4 * QK_RB + 512);
    }
  }
}
#define PIN_X(xf) asm volatile("" : "+v"(xf[0][0]), "+v"(xf[0][1]), "+v"(xf[1][0]), "+v"(xf[1][1]), "+v"(xf[2][0]), "+v"(xf[2][1]), "+v"(xf[3][0]), "+v"(xf[3][1]))
__device__ __forceinline__ void mma_a(f32x4 (&acc)[4][5], bf16x8 (&xf)[4][2], bf16x8 (&wa)[2][2]) {
  PIN_X(xf);
  __builtin_amdgcn_s_setprio(1);
#pragma unroll
  for (int kk = 0; kk < 2; ++kk)
#pragma unroll
    for (int mt = 0; mt < 4; ++mt)
#pragma unroll
      for (int s = 0; s < 2; ++s) acc[mt][s] = MFMA16(wa[s][kk], xf[mt][kk], acc[mt][s]);
  __builtin_amdgcn_s_setprio(0);
  asm volatile("" : "+v"(acc[0][0]), "+v"(acc[0][1]), "+v"(acc[1][0]), "+v"(acc[1][1]), "+v"(acc[2][0]), "+v"(acc[2][1]), "+v"(acc[3][0]), "+v"(acc[3][1]));
}
template <bool W3> __device__ __forceinline__ void mma_b(f32x4 (&acc)[4][5], bf16x8 (&xf)[4][2], bf16x8 (&wb)[3][2]) {
  PIN_X(xf);
  __builtin_amdgcn_s_setprio(1);
#pragma unroll
  for (int kk = 0; kk < 2; ++kk)
#pragma unroll
    for (int mt = 0; mt < 4; ++mt)
#pragma unroll
      for (int s = 0; s < 3; ++s) {
        if (W3 && s > 0) acc[mt][2 + s] = MFMA16(xf[mt][kk], wb[s][kk], acc[mt][2 + s]);
        else             acc[mt][2 + s] = MFMA16(wb[s][kk], xf[mt][kk], acc[mt][2 + s]);
      }
  __builtin_amdgcn_s_setprio(0);
  asm volatile("" : "+v"(acc[0][2]), "+v"(acc[0][3]), "+v"(acc[0][4]), "+v"(acc[1][2]), "+v"(acc[1][3]), "+v"(acc[1][4]),
                    "+v"(acc[2][2]), "+v"(acc[2][3]), "+v"(acc[2][4]), "+v"(acc[3][2]), "+v"(acc[3][3]), "+v"(acc[3][4]));
}

template <int ST, bool W3>
__device__ __forceinline__ void ktile(const Ctx& c, f32x4 (&acc)[2][4][5], bf16x8 (&xf)[4][2], bf16x8 (&wa)[2][2], bf16x8 (&wb)[3][2],
                                      unsigned kX, unsigned kG, unsigned kQ) {
  read_X<ST, 0>(c, xf); stage_Wa<ST>(c, kG);
  WAIT_VL(14); SCHED(); BAR(); SCHED();
  mma_a(acc[0], xf, wa);
  SCHED(); BAR(); SCHED();
  read_Wb<ST, W3>(c, wb); stage_X<ST, 0>(c, kX);
  WAIT_VL(14); SCHED(); BAR(); SCHED();
  mma_b<W3>(acc[0], xf, wb);
  SCHED(); BAR(); SCHED();
  read_X<ST, 1>(c, xf); stage_Wb<ST>(c, kG, kQ);
  WAIT_VL(16); SCHED(); BAR(); SCHED();
  mma_a(acc[1], xf, wa);
  SCHED(); BAR(); SCHED();
  read_Wa<ST ^ 1>(c, wa); stage_X<ST, 1>(c, kX);
  WAIT_VL(16); SCHED(); BAR(); SCHED();
  mma_b<W3>(acc[1], xf, wb);
  SCHED(); BAR(); SCHED();
}

constexpr int A_K = 66560, A_V = A_K + 32768, A_Q = A_V + 32768, A_CNT = A_Q + 16384;
template <bool W3>
__device__ __forceinline__ void body(const Ctx& c, LAS char* lds, int wid, int pm, int j, char* ws, const u16* wd, const u16* wo, u64& tag, const u16* x, const u16* gw) {
  const int wr = wid >> 2, wc = wid & 3;
  f32x4 acc[2][4][5];
#pragma unroll
  for (int a = 0; a < 2; ++a)
#pragma unroll
    for (int m = 0; m < 4; ++m)
#pragma unroll
      for (int s = 0; s < 5; ++s) acc[a][m][s] = (f32x4){0.f, 0.f, 0.f, 0.f};
  bf16x8 xf[4][2], wa[2][2], wb[3][2];
  const int grow = pm * 256 + j * 8 + wid;
  u32x4 gx0, gx1, gw0, gw1; unsigned epv;
  {
    asm volatile("global_load_dword %0, %1, off sc1" : "=v"(epv) : "v"(ws + WS_FLAG) : "memory");
    const unsigned ln_ = __builtin_amdgcn_mbcnt_hi(~0u, __builtin_amdgcn_mbcnt_lo(~0u, 0u));
    const char* xp = (const char*)(x + (size_t)grow * D_) + ln_ * 32; const char* gp = (const char*)gw + ln_ * 32;
    asm volatile("global_load_dwordx4 %0, %4, off\n\tglobal_load_dwordx4 %1, %4, off offset:16\n\tglobal_load_dwordx4 %2, %5, off\n\tglobal_load_dwordx4 %3, %5, off offset:16"
                 : "=&v"(gx0), "=&v"(gx1), "=&v"(gw0), "=&v"(gw1) : "v"(xp), "v"(gp) : "memory");
  }
  stage_Wa<0>(c, 0); stage_X<0, 0>(c, 0); stage_Wb<0>(c, 0, 0); stage_X<0, 1>(c, 0);
  stage_Wa<1>(c, 64u * 7168u); stage_X<1, 0>(c, 128); stage_Wb<1>(c, 64u * 7168u, 64u * 6144u); stage_X<1, 1>(c, 128);
  asm volatile("s_waitcnt vmcnt(20)" : "+v"(gx0), "+v"(gx1), "+v"(gw0), "+v"(gw1), "+v"(epv) :: "memory");
  {
    unsigned ep = __builtin_amdgcn_readfirstlane(epv) + 1u;
    asm volatile("" : "+s"(ep));
    tag = ((u64)ep << 32) | (u64)(ep ^ 0x9E3779B9u);
    float s = 0.f;
#pragma unroll
    for (int e = 0; e < 4; ++e) s += bf_lo(gx0[e]) * bf_lo(gw0[e]) + bf_hi(gx0[e]) * bf_hi(gw0[e]) + bf_lo(gx1[e]) * bf_lo(gw1[e]) + bf_hi(gx1[e]) * bf_hi(gw1[e]);
#pragma unroll
    for (int o = 32; o > 0; o >>= 1) s += __shfl_xor(s, o);
    const float g = 1.0f / (1.0f + __expf(-s));
    if (__builtin_amdgcn_mbcnt_hi(~0u, __builtin_amdgcn_mbcnt_lo(~0u, 0u)) == 0)
      __hip_atomic_store((u64*)(ws + WS_G) + grow, (tag & 0xffffffff00000000ull) | (u64)__float_as_uint(g), __ATOMIC_RELAXED, __HIP_MEMORY_SCOPE_AGENT);
  }
  SCHED();
  WAIT_V(17); BAR();
  if (wr == 1) BAR();
  read_Wa<0>(c, wa);
  WAIT_L0(); SCHED(); BAR(); SCHED(); BAR(); SCHED();
#pragma unroll 1
  for (int t = 0; t < 16; t += 2) {
    if (t == 14) {
      const unsigned ln_ = __builtin_amdgcn_mbcnt_hi(~0u, __builtin_amdgcn_mbcnt_lo(~0u, 0u));
      const unsigned gl = (ln_ * 2048u + blockIdx.x * 8u + (unsigned)wid) * 128u;
      touch(c.mS, gl, mk_rsrc(wd, F_ * D_ * 2)); touch(c.mS, gl, mk_rsrc(wo, D_ * D_ * 2));
    }
    const bool more = t < 14;
    unsigned kX = more ? (unsigned)(t + 2) * 128u : OOB;
    unsigned kG = more ? (unsigned)(t + 2) * 64u * 7168u : OOB;
    unsigned kQ = more ? (unsigned)(t + 2) * 64u * 6144u : OOB;
    ktile<0, W3>(c, acc, xf, wa, wb, kX, kG, kQ);
    ktile<1, W3>(c, acc, xf, wa, wb, kX + 128u, kG + 64u * 7168u, kQ + 64u * 6144u);
  }
  if (wr == 0) BAR();
  WAIT_V(0); BAR();
  STAMP_AT(1);
  int lane = __builtin_amdgcn_mbcnt_hi(~0u, __builtin_amdgcn_mbcnt_lo(~0u, 0u)); asm volatile("" : "+v"(lane));
  const int fr = lane & 15, fq = lane >> 4;
  {
    const int qk = wr * 2 * 2048 + (fq >> 1) * 512 + fr * 16 + (fq & 1) * 8;
    if constexpr (!W3) {
      LAS char* p = lds + (wc == 2 ? A_K + 16384 : A_Q) + qk + (wc & 1) * 1024;
#pragma unroll
      for (int a = 0; a < 2; ++a)
#pragma unroll
        for (int m = 0; m < 4; ++m) {
          const f32x4 v = acc[a][m][4];
          u32x2 w; w.x = pk_bf16(v[0], v[1]); w.y = pk_bf16(v[2], v[3]);
          *(LAS u32x2*)(p + (a * 4 + (m >> 1)) * 2048 + (m & 1) * 256) = w;
        }
    } else {
      LAS char* pk = lds + A_K + 16384 + qk + 1024;
      LAS char* pv = lds + A_V + 16384 + wr * 4 * 1024 + (fq & 1) * 512 + fr * 16 + (fq >> 1) * 8;
#pragma unroll
      for (int a = 0; a < 2; ++a)
#pragma unroll
        for (int m = 0; m < 4; ++m) {
          const f32x4 v = acc[a][m][2], v0 = acc[a][m][3], v1 = acc[a][m][4];
          u32x2 w; w.x = pk_bf16(v[0], v[1]); w.y = pk_bf16(v[2], v[3]);
          *(LAS u32x2*)(pk + (a * 4 + (m >> 1)) * 2048 + (m & 1) * 256) = w;
          u32x2 w0; w0.x = pk_bf16(v0[0], v0[1]); w0.y = pk_bf16(v0[2], v0[3]);
          u32x2 w1; w1.x = pk_bf16(v1[0], v1[1]); w1.y = pk_bf16(v1[2], v1[3]);
          *(LAS u32x2*)(pv + (a * 8 + m) * 1024) = w0;
          *(LAS u32x2*)(pv + (a * 8 + m) * 1024 + 256) = w1;
        }
    }
  }
  if (wid == 0 && lane < 4) ((LAS unsigned*)(lds + A_CNT))[lane] = 0u;
  WAIT_L0(); BAR();
  {
    const rsrc_t sKV = mk_rsrc(ws + WS_KV + (size_t)(pm * 32 + j) * 32768, 32768);
    LAS char* src = lds + (wid < 4 ? A_K + 16384 + wid * 4096 : A_V + 16384 + (wid - 4) * 4096) + lane * 16;
    u32x4 t[4];
#pragma unroll
    for (int i = 0; i < 4; ++i) t[i] = *(LAS u32x4*)(src + i * 1024);
#pragma unroll
    for (int i = 0; i < 4; ++i) __builtin_amdgcn_raw_buffer_store_b128(t[i], sKV, (unsigned)(wid * 4096 + lane * 16 + i * 1024), 0, 16);
  }
  SCHED();
  const rsrc_t sA = mk_rsrc(ws + WS_A2, A2_BYTES);
  const unsigned hoff = (unsigned)((((j >> 2) * 28 + (j & 3) * 4 + wc) * 2048 + pm * 256 + wr * 64 + (fq & 1) * 16 + fr) * 32 + (fq >> 1) * 16);
  auto hstore = [&](int a, int mp, int sg, unsigned so) {
    const f32x4 g0 = acc[a][2 * mp][sg], u0 = acc[a][2 * mp][sg + 1], g1 = acc[a][2 * mp + 1][sg], u1 = acc[a][2 * mp + 1][sg + 1];
    const unsigned a0 = geglu2(g0[0], g0[1], u0[0], u0[1]), a1 = geglu2(g0[2], g0[3], u0[2], u0[3]);
    const unsigned b0 = geglu2(g1[0], g1[1], u1[0], u1[1]), b1 = geglu2(g1[2], g1[3], u1[2], u1[3]);
    auto r0 = __builtin_amdgcn_permlane16_swap(a0, b0, false, false);
    auto r1 = __builtin_amdgcn_permlane16_swap(a1, b1, false, false);
    u32x4 w; w.x = r0[0]; w.y = r1[0]; w.z = r0[1]; w.w = r1[1];
    __builtin_amdgcn_raw_buffer_store_b128(w, sA, hoff + so + (unsigned)((a * 128 + mp * 32) * 32), 0, 16);
  };
  hstore(0, 0, 0, 0); hstore(0, 1, 0, 0); hstore(1, 0, 0, 0); hstore(1, 1, 0, 0);
  SCHED();
  WAIT_V(4);
  if (arrive8((LAS unsigned*)(lds + A_CNT), lane) && lane == 0)
    __hip_atomic_store((u64*)(ws + WS_FLAG + FL_KV) + pm * 32 + j, tag, __ATOMIC_RELAXED, __HIP_MEMORY_SCOPE_AGENT);
  SCHED();
  if constexpr (!W3) {
    const unsigned d2 = (unsigned)(16 - (j & 3)) * 65536u;
    hstore(0, 0, 2, d2); hstore(0, 1, 2, d2); hstore(1, 0, 2, d2); hstore(1, 1, 2, d2);
  }
}
}

namespace k2 {
constexpr int B_RB = 1024 + 32, B_H = 8 * B_RB, ST_A = 16384, ST_B = 2 * B_H, ST = ST_A + ST_B;
constexpr int NOTE = 4 * ST;
__device__ __forceinline__ bf16x8 lds_tr2b(LAS char* p) {
  s16x4 a = __builtin_amdgcn_ds_read_tr16_b64_v4i16((LAS s16x4*)p);
  s16x4 b = __builtin_amdgcn_ds_read_tr16_b64_v4i16((LAS s16x4*)(p + 512));
  s16x8 r = __builtin_shufflevector(a, b, 0, 1, 2, 3, 4, 5, 6, 7);
  return __builtin_bit_cast(bf16x8, r);
}
__device__ __forceinline__ void wait_tags(const u64* fl, int n, u64 tag, int lane) {
  const u64* p = fl + (lane < n ? lane : 0);
  for (unsigned spins = 0; spins < (1u << 20); ++spins) {
    if (__all(__hip_atomic_load(p, __ATOMIC_RELAXED, __HIP_MEMORY_SCOPE_AGENT) == tag)) break;
    __builtin_amdgcn_s_sleep(1);
  }
}
__device__ __forceinline__ float read_gate(const char* ws, int row, u64 tag) {
  const u64* p = (const u64*)(ws + WS_G) + row;
  u64 v = 0;
  for (unsigned spins = 0; spins < (1u << 20); ++spins) {
    v = __hip_atomic_load(p, __ATOMIC_RELAXED, __HIP_MEMORY_SCOPE_AGENT);
    if (__all((v >> 32) == (tag >> 32))) break;
    __builtin_amdgcn_s_sleep(1);
  }
  return __uint_as_float((unsigned)v);
}
#define ACQUIRE() do { __builtin_amdgcn_fence(__ATOMIC_ACQUIRE, "agent"); asm volatile("s_waitcnt vmcnt(0)" ::: "memory"); } while (0)
template <bool OWN, class Hook>
__device__ __forceinline__ void attn_chunk(LAS char* kl, LAS char* vl, bf16x8 qf0, bf16x8 qf1, int cnt, int l31, int hh, float& mrun, f32x2& lrun, f32x16& oacc, Hook hook) {
  const float NEG = -__builtin_inff();
  const float cs = 0.17677669529663687f * 1.4426950408889634f;
  f32x16 sc[8];
  float mx = mrun;
#pragma unroll
  for (int e = 0; e < 8; ++e) {
    if (e < cnt) {
      const int i = OWN ? 8 - e : e;
      f32x16 z = {};
      z = MFMA32(lds_b128(kl + i * 2048), qf0, z);
      z = MFMA32(lds_b128(kl + i * 2048 + 1024), qf1, z);
      if (e == 0) {
#pragma unroll
        for (int r = 0; r < 16; ++r) { const int kl_ = (r & 3) + 8 * (r >> 2) + 4 * hh; z[r] = (OWN != (kl_ > l31)) ? z[r] : NEG; }
      }
      float ma = fmaxf(z[0], z[1]), mb = fmaxf(z[2], z[3]);
#pragma unroll
      for (int r = 4; r < 16; r += 4) { ma = fmaxf(fmaxf(ma, z[r]), z[r + 1]); mb = fmaxf(fmaxf(mb, z[r + 2]), z[r + 3]); }
      mx = fmaxf(mx, fmaxf(ma, mb));
      sc[e] = z;
    }
  }
  mx = fmaxf(mx, __shfl_xor(mx, 32));
  hook();
  if constexpr (!OWN) {
    const float al = __builtin_amdgcn_exp2f((mrun - mx) * cs);
    lrun *= al;
#pragma unroll
    for (int r = 0; r < 16; ++r) oacc[r] *= al;
  }
  mrun = mx;
  const f32x2 cs2 = {cs, cs}, mc2 = {-mx * cs, -mx * cs};
  f32x2 l2[2] = {lrun, {0.f, 0.f}};
#pragma unroll
  for (int e = 0; e < 8; ++e) {
    if (e < cnt) {
      const int i = OWN ? 8 - e : e;
#pragma unroll
      for (int s = 0; s < 2; ++s) {
        u32x4 pw;
#pragma unroll
        for (int k = 0; k < 4; ++k) {
          const f32x2 sv = {sc[e][8 * s + 2 * k], sc[e][8 * s + 2 * k + 1]};
          const f32x2 t = __builtin_elementwise_fma(sv, cs2, mc2);
          f32x2 p; p.x = __builtin_amdgcn_exp2f(t.x); p.y = __builtin_amdgcn_exp2f(t.y);
          l2[k & 1] += p;
          pw[k] = pk_bf16(p.x, p.y);
        }
        oacc = MFMA32(lds_b128(vl + i * 2048 + s * 1024), __builtin_bit_cast(bf16x8, pw), oacc);
      }
    }
  }
  lrun = l2[0] + l2[1];
}
__device__ __forceinline__ void attn_finish(const f32x16& oacc, f32x2 lrun, int row0, int lane, rsrc_t sA, int head, float gq) {
  const int l31 = lane & 31, hh = lane >> 5;
  float sum = lrun.x + lrun.y;
  sum += __shfl_xor(sum, 32);
  const float inv = gq / sum;
#pragma unroll
  for (int g2 = 0; g2 < 2; ++g2) {
    unsigned a0 = pk_bf16(oacc[8 * g2 + 0] * inv, oacc[8 * g2 + 1] * inv), a1 = pk_bf16(oacc[8 * g2 + 2] * inv, oacc[8 * g2 + 3] * inv);
    unsigned b0 = pk_bf16(oacc[8 * g2 + 4] * inv, oacc[8 * g2 + 5] * inv), b1 = pk_bf16(oacc[8 * g2 + 6] * inv, oacc[8 * g2 + 7] * inv);
    auto r0 = __builtin_amdgcn_permlane32_swap(a0, b0, false, false);
    auto r1 = __builtin_amdgcn_permlane32_swap(a1, b1, false, false);
    u32x4 w; w.x = r0[0]; w.y = r1[0]; w.z = r0[1]; w.w = r1[1];
    __builtin_amdgcn_raw_buffer_store_b128(w, sA, (unsigned)(((224 + head * 2 + g2) * 2048 + row0 + l31) * 32 + hh * 16), 0, 16);
  }
}

template <int MC> __device__ __forceinline__ void dma4k(unsigned mbase, unsigned voff, rsrc_t srd, unsigned soff) {
  asm volatile("s_add_u32 m0, %0, %4\n\ts_nop 0\n\tbuffer_load_dwordx4 %1, %2, %3 offen lds\n\tbuffer_load_dwordx4 %1, %2, %3 offen offset:1024 lds\n\t"
               "buffer_load_dwordx4 %1, %2, %3 offen offset:2048 lds\n\tbuffer_load_dwordx4 %1, %2, %3 offen offset:3072 lds"
               :: "s"(mbase), "v"(voff), "s"(srd), "s"(soff), "i"(MC) : "memory", "scc");
}

struct Ctx { LAS char* ar; LAS char* br0; LAS char* br1; LAS char* ar2; LAS char* br02; LAS char* br12; unsigned vA0, vA1, vB, mA, mB; rsrc_t sA; };
template <int SL, int I> __device__ __forceinline__ void piece(const Ctx& c, rsrc_t sB, unsigned ga, unsigned kb) {
  if constexpr (I == 0) dma64<SL * ST>(c.mA, c.vA0, c.sA, ga);
  if constexpr (I == 1) dma64<SL * ST + 8192>(c.mA, c.vA1, c.sA, ga);
  if constexpr (I == 2) dma64<SL * ST>(c.mB, c.vB, sB, kb);
  if constexpr (I == 3) dma64_o128<SL * ST + B_H>(c.mB, c.vB, sB, kb);
}
template <int SL> __device__ __forceinline__ void stage(const Ctx& c, rsrc_t sB, unsigned ga, unsigned kb) {
  piece<SL, 0>(c, sB, ga, kb); piece<SL, 1>(c, sB, ga, kb); piece<SL, 2>(c, sB, ga, kb); piece<SL, 3>(c, sB, ga, kb);
}
template <int SL, int M, int KG>
__device__ __forceinline__ void quarter(const Ctx& c, f32x4 (&acc)[4][4], bf16x8 (&af)[4], bf16x8 (&bf)[4], bf16x8 (&an)[4], bf16x8 (&bn)[4], rsrc_t sB, unsigned ga, unsigned kb) {
  constexpr int ns = (SL + 1) & 3, so = (ns & 1) * ST;
  LAS char* const ar = (ns & 2) ? c.ar2 : c.ar; LAS char* const br0 = (ns & 2) ? c.br02 : c.br0; LAS char* const br1 = (ns & 2) ? c.br12 : c.br1;
  if constexpr (KG == 1) piece<SL, M>(c, sB, ga, kb);
  SCHED();
  asm volatile("" : "+v"(af[M]));
#pragma unroll
  for (int n = 0; n < 4; ++n) acc[M][n] = MFMA16(bf[n], af[M], acc[M][n]);
  asm volatile("" : "+v"(acc[M][0]), "+v"(acc[M][1]), "+v"(acc[M][2]), "+v"(acc[M][3]));
  SCHED();
  if constexpr (M < 2) {
    bn[2 * M] = lds_tr2b((M ? br1 : br0) + so); bn[2 * M + 1] = lds_tr2b((M ? br1 : br0) + so + 32);
    an[2 * M] = lds_b128(ar + so + 2 * M * 512);
  }
  if constexpr (M == 2) { an[1] = lds_b128(ar + so + 512); an[3] = lds_b128(ar + so + 3 * 512); }
  if constexpr (KG == 0) piece<SL, M>(c, sB, ga, kb);
  SCHED();
}
template <int SL, int KG>
__device__ __forceinline__ void step(const Ctx& c, f32x4 (&acc)[4][4], bf16x8 (&af)[4], bf16x8 (&bf)[4], bf16x8 (&an)[4], bf16x8 (&bn)[4], rsrc_t sB, unsigned ga, unsigned kb) {
  quarter<SL, 0, KG>(c, acc, af, bf, an, bn, sB, ga, kb); quarter<SL, 1, KG>(c, acc, af, bf, an, bn, sB, ga, kb);
  quarter<SL, 2, KG>(c, acc, af, bf, an, bn, sB, ga, kb); quarter<SL, 3, KG>(c, acc, af, bf, an, bn, sB, ga, kb);
  WAIT_VL(8); SCHED(); BAR(); SCHED();
}
template <int KG>
__device__ __forceinline__ void gemm_loop(const Ctx& c, f32x4 (&acc)[4][4], float (&og)[4], rsrc_t sB, rsrc_t sO, unsigned ga, unsigned kb, int q_sw, bool poller,
                                          const u64* fo, u64 tag, int lane) {
  bf16x8 af0[4], bf0[4], af1[4], bf1[4];
  bf0[0] = lds_tr2b(c.br0); bf0[1] = lds_tr2b(c.br0 + 32); bf0[2] = lds_tr2b(c.br1); bf0[3] = lds_tr2b(c.br1 + 32);
#pragma unroll
  for (int m = 0; m < 4; ++m) af0[m] = lds_b128(c.ar + m * 512);
  WAIT_L0(); SCHED(); BAR(); SCHED();
#pragma unroll 1
  for (int q = 0; q < 9; ++q) {
    if (q == q_sw - 1 && poller) { wait_tags(fo, 32, tag, lane); ACQUIRE(); }
    if (q == q_sw) { sB = sO; kb = 0; }
    if (q == q_sw + 1) {
      asm volatile("" : "+v"(og[0]), "+v"(og[1]), "+v"(og[2]), "+v"(og[3]));
#pragma unroll
      for (int m = 0; m < 4; ++m)
#pragma unroll
        for (int n = 0; n < 4; ++n) acc[m][n] *= og[m];
    }
    step<0, KG>(c, acc, af0, bf0, af1, bf1, sB, ga, kb);
    step<1, KG>(c, acc, af1, bf1, af0, bf0, sB, ga + 262144u, kb + 131072u);
    step<2, KG>(c, acc, af0, bf0, af1, bf1, sB, ga + 2u * 262144u, kb + 2u * 131072u);
    step<3, KG>(c, acc, af1, bf1, af0, bf0, sB, ga + 3u * 262144u, kb + 3u * 131072u);
    ga += 4u * 262144u; kb += 4u * 131072u;
  }
}
}

__global__ __launch_bounds__(512, 2) void fused_kernel(const u16* __restrict__ x, const u16* __restrict__ wqkv, const u16* __restrict__ wg, const u16* __restrict__ wu,
                                                       const u16* __restrict__ gw, const u16* __restrict__ wd, const u16* __restrict__ wo, char* __restrict__ ws,
                                                       u16* __restrict__ out) {
  extern __shared__ __attribute__((aligned(16))) char smem[];
  LAS char* lds = (LAS char*)smem;
  const unsigned lbase = (unsigned)(size_t)lds;
  const int wid = __builtin_amdgcn_readfirstlane(threadIdx.x >> 6);
  const int bid = blockIdx.x, xcd = bid & 7, idx = bid >> 3;
  const int j = xcd * 4 + (idx >> 3), pm = idx & 7;
  u64 tag;
  STAMP_AT(0);
  {
    using namespace k1;
    const int lane = threadIdx.x & 63, wr = wid >> 2, wc = wid & 3;
    Ctx c;
    c.sX = mk_rsrc(x, S_ * D_ * 2);
    c.sG = mk_rsrc(wg, D_ * F_ * 2);
    c.sU = mk_rsrc(wu, D_ * F_ * 2);
    c.sQ = mk_rsrc(wqkv, D_ * 3072 * 2);
    c.sV = mk_rsrc((const char*)wqkv + 4096, D_ * 3072 * 2 - 4096);
    const int r = lane >> 3, cp = lane & 7, ch = (cp & 4) | ((cp & 3) ^ ((r >> 1) & 3));
    c.vX = (unsigned)((pm * 256 + wid * 8 + r) * 2048 + ch * 16);
    const int u = lane >> 4, rr = (lane >> 1) & 7, hf = lane & 1;
    c.vG = (unsigned)((wid * 8 + rr) * 7168 + (j >> 2) * 896 + (j & 3) * 128 + u * 32 + hf * 16);
    c.dB = (unsigned)(512 - 32 * (j & 3));
    c.vQ = (unsigned)((wid * 8 + rr) * 6144 + (u >> 1) * 2048 + j * 64 + (u & 1) * 32 + hf * 16);
    c.mS = lbase + SCR; c.mX = lbase + wid * XP; c.mG = lbase + wid * GU_RB; c.mQ = lbase + wid * QK_RB;
    const int fr = lane & 15, q = lane >> 4;
    c.xr = lds + X0 + (wr * 8 + (fr >> 3)) * XP + (fr & 7) * 128 + ((q ^ ((fr >> 1) & 3)) * 16);
    const int qq = (lane & 15) >> 2, p = lane & 3;
    c.gr = lds + GU0 + q * GU_RB + qq * 32 + p * 8 + wc * 256;
    c.qr = lds + QK0 + q * QK_RB + qq * 32 + p * 8 + wc * 256;
    if (wc == 3) body<true>(c, lds, wid, pm, j, ws, wd, wo, tag, x, gw); else body<false>(c, lds, wid, pm, j, ws, wd, wo, tag, x, gw);
  }
  using namespace k2;
  int lane = __builtin_amdgcn_mbcnt_hi(~0u, __builtin_amdgcn_mbcnt_lo(~0u, 0u)); asm volatile("" : "+v"(lane));
  STAMP_AT(2);
  const int hb = xcd & 1;
  const int mt = (xcd >> 2) * 8 + (idx >> 2), nt = ((xcd >> 1) & 1) * 4 + (idx & 3), panel = mt >> 1;
  const rsrc_t sA3 = mk_rsrc(ws + WS_A2, hb ? A2_BYTES : 36u * 262144u);
  const rsrc_t sD = mk_rsrc(wd, hb ? F_ * D_ * 2 : 36 * 131072), sO = mk_rsrc(wo, D_ * D_ * 2);
  const unsigned ga0 = hb ? 36u * 262144u : 0u, kb0 = hb ? 36u * 131072u : 0u;
  float g4;
  {
    const int ql = wid < 4 ? wid : 11 - wid;
    const int l31 = lane & 31, hh = lane >> 5;
    LAS char* kl = lds + k1::A_K + ql * 2048 + lane * 16; LAS char* vl = lds + k1::A_V + ql * 2048 + lane * 16;
    const bf16x8 qf0 = lds_b128(lds + k1::A_Q + ql * 2048 + lane * 16), qf1 = lds_b128(lds + k1::A_Q + ql * 2048 + 1024 + lane * 16);
    float mrun = -__builtin_inff(); f32x2 lrun = {0.f, 0.f}; f32x16 oacc = {};
    auto h_done = [&]() {
      WAIT_V(0);
      if (arrive8((LAS unsigned*)(lds + k1::A_CNT) + 1, lane) && lane == 0)
        __hip_atomic_store((u64*)(ws + WS_FLAG + FL_H) + pm * 32 + j, tag, __ATOMIC_RELAXED, __HIP_MEMORY_SCOPE_AGENT);
    };
    if (wid == 7) h_done();
    if (wid == 7 && pm > 0) {
      wait_tags((const u64*)(ws + WS_FLAG + FL_KV) + (pm - 1) * 32 + j, 1, tag, lane);
      __builtin_amdgcn_fence(__ATOMIC_ACQUIRE, "agent");
      const rsrc_t sKV = mk_rsrc(ws + WS_KV + (size_t)((pm - 1) * 32 + j) * 32768, 32768);
      const unsigned vl_ = (unsigned)(lane * 16);
      dma4k<k1::A_K>(lbase, vl_, sKV, 0u); dma4k<k1::A_K + 4096>(lbase, vl_, sKV, 4096u); dma4k<k1::A_K + 8192>(lbase, vl_, sKV, 8192u); dma4k<k1::A_K + 12288>(lbase, vl_, sKV, 12288u);
      dma4k<k1::A_V>(lbase, vl_, sKV, 16384u); dma4k<k1::A_V + 4096>(lbase, vl_, sKV, 20480u); dma4k<k1::A_V + 8192>(lbase, vl_, sKV, 24576u); dma4k<k1::A_V + 12288>(lbase, vl_, sKV, 28672u);
    }
    float gq;
    attn_chunk<true>(kl, vl, qf0, qf1, ql + 1, l31, hh, mrun, lrun, oacc, [&]() {
      gq = read_gate(ws, pm * 256 + ql * 32 + l31, tag);
      g4 = 1.0f - read_gate(ws, mt * 128 + ((wid >> 1) & 1) * 64 + lane, tag);
      if (wid != 7) h_done(); else WAIT_V(0);
    });
    STAMP_AT(3);
    BAR();
    STAMP_AT(4);
    if (pm > 0) attn_chunk<false>(kl, vl, qf0, qf1, 8 - ql, l31, hh, mrun, lrun, oacc, []() {});
    attn_finish(oacc, lrun, pm * 256 + ql * 32, lane, mk_rsrc(ws + WS_A2, A2_BYTES), j, gq);
    if (wid == 4) {
      wait_tags((const u64*)(ws + WS_FLAG + FL_H) + panel * 32, 32, tag, lane);
      __builtin_amdgcn_fence(__ATOMIC_ACQUIRE, "agent");
      const unsigned vA = (unsigned)(mt * 128 * 32 + lane * 16);
      const unsigned vB = (unsigned)((lane >> 3) * 2048 + nt * 256 + (((lane & 7) ^ ((lane >> 2) & 4)) * 16));
#pragma unroll 1
      for (int t = 0; t < 2; ++t) {
        const unsigned mb = lbase + t * ST;
#pragma unroll
        for (int g = 0; g < 4; ++g) dma4k<0>(mb + g * 4096, vA, sA3, ga0 + t * 262144u + g * 65536u);
#pragma unroll 1
        for (int rb = 0; rb < 8; ++rb) {
          dma64<ST_A>(mb + rb * B_RB, vB, sD, kb0 + t * 131072u + rb * 16384u);
          dma64_o128<ST_A + B_H>(mb + rb * B_RB, vB, sD, kb0 + t * 131072u + rb * 16384u);
        }
      }
    }
  }
  STAMP_AT(5);
  WAIT_VL(0); BAR();
  if (wid == 0 && lane == 0) __hip_atomic_store((u64*)(ws + WS_FLAG + FL_O) + pm * 32 + j, tag, __ATOMIC_RELAXED, __HIP_MEMORY_SCOPE_AGENT);
  STAMP_AT(6);
  const int kg = wid >> 2, wm = (wid >> 1) & 1, wn = wid & 1;
  const int fr = lane & 15, fq = lane >> 4;
  Ctx c;
  c.sA = sA3;
  c.vA0 = (unsigned)(((wid >> 2) * 2048 + mt * 128 + (wid & 3) * 32) * 32 + lane * 16); c.vA1 = c.vA0 + 131072u;
  c.vB = (unsigned)((wid * 8 + (lane >> 3)) * 2048 + nt * 256 + (((lane & 7) ^ ((lane >> 2) & 4)) * 16));
  c.mA = lbase + wid * 1024; c.mB = lbase + ST_A + wid * B_RB;
  float og[4];
#pragma unroll
  for (int m = 0; m < 4; ++m) og[m] = __shfl(g4, m * 16 + fr);
  asm volatile("" : "+v"(og[0]), "+v"(og[1]), "+v"(og[2]), "+v"(og[3]));
  stage<2>(c, sD, ga0 + 2u * 262144u, kb0 + 2u * 131072u); stage<3>(c, sD, ga0 + 3u * 262144u, kb0 + 3u * 131072u);
  c.ar = lds + (kg * 2 + (fq >> 1)) * 4096 + (wm * 64 + fr) * 32 + (fq & 1) * 16;
  c.br0 = lds + ST_A + wn * B_H + (kg * 4 + fq) * B_RB + (fr >> 2) * 128 + ((fr >> 3) * 64) + (lane & 3) * 8;
  c.br1 = lds + ST_A + wn * B_H + (kg * 4 + fq) * B_RB + (fr >> 2) * 128 + (((fr >> 3) ^ 1) * 64) + (lane & 3) * 8;
  c.ar2 = c.ar + 2 * ST; c.br02 = c.br0 + 2 * ST; c.br12 = c.br1 + 2 * ST;
  asm volatile("" : "+v"(c.ar2), "+v"(c.br02), "+v"(c.br12));
  f32x4 acc[4][4];
#pragma unroll
  for (int m = 0; m < 4; ++m)
#pragma unroll
    for (int n = 0; n < 4; ++n) acc[m][n] = (f32x4){0.f, 0.f, 0.f, 0.f};
  STAMP_AT(7);
  {
    const int q_sw = hb ? 4 : 1000;
    const u64* fo = (const u64*)(ws + WS_FLAG + FL_O) + panel * 32;
    if (kg == 0) gemm_loop<0>(c, acc, og, sD, sO, ga0 + 4u * 262144u, kb0 + 4u * 131072u, q_sw, wid == 0, fo, tag, lane);
    else         gemm_loop<1>(c, acc, og, sD, sO, ga0 + 4u * 262144u, kb0 + 4u * 131072u, q_sw, false, fo, tag, lane);
  }
  WAIT_V(0); BAR();
  STAMP_AT(8);
  if (hb == 0) {
#pragma unroll
    for (int m = 0; m < 4; ++m)
#pragma unroll
      for (int n = 0; n < 4; ++n) acc[m][n] *= og[m];
  }
  LAS f32x4* red = (LAS f32x4*)lds + lane;
  f32x4 r[2][4];
  if (kg == 0) {
#pragma unroll
    for (int m = 0; m < 2; ++m)
#pragma unroll
      for (int n = 0; n < 4; ++n) { red[(wid * 8 + m * 4 + n) * 64] = acc[2 + m][n]; r[m][n] = acc[m][n]; }
  } else {
#pragma unroll
    for (int m = 0; m < 2; ++m)
#pragma unroll
      for (int n = 0; n < 4; ++n) { red[(wid * 8 + m * 4 + n) * 64] = acc[m][n]; r[m][n] = acc[2 + m][n]; }
  }
  WAIT_L0(); BAR();
#pragma unroll
  for (int m = 0; m < 2; ++m)
#pragma unroll
    for (int n = 0; n < 4; ++n) r[m][n] += red[((wid ^ 4) * 8 + m * 4 + n) * 64];
  {
    const rsrc_t sPo = mk_rsrc(ws + WS_PART + (size_t)bid * 32768, 32768), sPi = mk_rsrc(ws + WS_PART + (size_t)(bid ^ 1) * 32768, 32768);
    f32x4 kp[4];
    if (hb) {
#pragma unroll
      for (int n = 0; n < 4; ++n) { __builtin_amdgcn_raw_buffer_store_b128(__builtin_bit_cast(u32x4, r[0][n]), sPo, (unsigned)(wid * 4096 + lane * 16 + n * 1024), 0, 16); kp[n] = r[1][n]; }
      asm volatile("" : "+v"(kp[0]), "+v"(kp[1]), "+v"(kp[2]), "+v"(kp[3]));
    } else {
#pragma unroll
      for (int n = 0; n < 4; ++n) { __builtin_amdgcn_raw_buffer_store_b128(__builtin_bit_cast(u32x4, r[1][n]), sPo, (unsigned)(wid * 4096 + lane * 16 + n * 1024), 0, 16); kp[n] = r[0][n]; }
      asm volatile("" : "+v"(kp[0]), "+v"(kp[1]), "+v"(kp[2]), "+v"(kp[3]));
    }
    WAIT_V(0); BAR();
    if (wid == 0) {
      if (lane == 0) __hip_atomic_store((u64*)(ws + WS_FLAG + FL_P) + bid * 8, tag, __ATOMIC_RELAXED, __HIP_MEMORY_SCOPE_AGENT);
      wait_tags((const u64*)(ws + WS_FLAG + FL_P) + (bid ^ 1) * 8, 1, tag, lane);
    }
    BAR();
    STAMP_AT(9);
    u16* op = out + (size_t)(mt * 128 + wm * 64 + kg * 32 + hb * 16 + fr) * D_ + nt * 128 + wn * 64 + fq * 4;
    f32x4 pp[4];
#pragma unroll
    for (int n = 0; n < 4; ++n)
      pp[n] = __builtin_bit_cast(f32x4, __builtin_amdgcn_raw_buffer_load_b128(sPi, (unsigned)(lane * 16 + n * 1024), (unsigned)(wid * 4096), 16));
#pragma unroll
    for (int n = 0; n < 4; ++n) {
      const f32x4 v = kp[n] + pp[n];
      u32x2 w; w.x = pk_bf16(v[0], v[1]); w.y = pk_bf16(v[2], v[3]);
      *(u32x2*)(op + n * 16) = w;
    }
  }
  if (bid == 0 && wid == 0) {
#pragma unroll 1
    for (int i = 0; i < 4; ++i) wait_tags((const u64*)(ws + WS_FLAG + FL_H) + i * 64, 64, tag, lane);
    if (lane == 0) __hip_atomic_store((unsigned*)(ws + WS_FLAG), (unsigned)(tag >> 32), __ATOMIC_RELAXED, __HIP_MEMORY_SCOPE_AGENT);
  }
  STAMP_AT(10);
}

extern "C" void kernel_launch(void* const* d_in, const int* in_sizes, int n_in, void* d_out, int out_size, void* d_ws, size_t ws_size,
                              hipStream_t stream) {
  const u16* x = (const u16*)d_in[0]; const u16* wqkv = (const u16*)d_in[1]; const u16* wo = (const u16*)d_in[2];
  const u16* wg = (const u16*)d_in[4]; const u16* wu = (const u16*)d_in[5]; const u16* wd = (const u16*)d_in[6]; const u16* gw = (const u16*)d_in[7];
  (void)hipFuncSetAttribute((const void*)fused_kernel, hipFuncAttributeMaxDynamicSharedMemorySize, k1::LDS_BYTES);
  fused_kernel<<<256, 512, k1::LDS_BYTES, stream>>>(x, wqkv, wg, wu, gw, wd, wo, (char*)d_ws, (u16*)d_out);
}
```

```cpp
#include <hip/hip_runtime.h>
#include <hip/hip_bf16.h>

#define LAS __attribute__((address_space(3)))
typedef unsigned short u16;
typedef short s16x4 __attribute__((ext_vector_type(4)));
typedef short s16x8 __attribute__((ext_vector_type(8)));
typedef __bf16 bf16x8 __attribute__((ext_vector_type(8)));
typedef __bf16 bf16x2 __attribute__((ext_vector_type(2)));
typedef float f32x2 __attribute__((ext_vector_type(2)));
typedef float f32x4 __attribute__((ext_vector_type(4)));
typedef float f32x16 __attribute__((ext_vector_type(16)));
typedef unsigned u32x2 __attribute__((ext_vector_type(2)));
typedef unsigned u32x4 __attribute__((ext_vector_type(4)));
typedef __amdgpu_buffer_rsrc_t rsrc_t;

constexpr int S_ = 2048, D_ = 1024, F_ = 3584;
typedef unsigned long long u64;
constexpr size_t WS_A2 = 0;
constexpr size_t WS_KV = 20u << 20;
constexpr size_t WS_G  = 32u << 20;
constexpr size_t WS_FLAG = 33u << 20;
constexpr size_t WS_PART = 40u << 20;
constexpr unsigned A2_BYTES = 288u * 2048u * 32u;
constexpr unsigned FL_KV = 4096, FL_H = 8192, FL_O = 12288, FL_P = 16384;

#define BAR() do { asm volatile("" ::: "memory"); __builtin_amdgcn_s_barrier(); asm volatile("" ::: "memory"); } while (0)
#define SCHED() __builtin_amdgcn_sched_barrier(0)
#define WAIT_VL(n) do { asm volatile("s_waitcnt vmcnt(" #n ")" ::: "memory"); __builtin_amdgcn_s_waitcnt(0xC07F); } while (0)
#define WAIT_V(n) asm volatile("s_waitcnt vmcnt(" #n ")" ::: "memory")
#define WAIT_L0() do { asm volatile("" ::: "memory"); __builtin_amdgcn_s_waitcnt(0xC07F); } while (0)

__device__ __forceinline__ rsrc_t mk_rsrc(const void* p, unsigned bytes) {
  return __builtin_amdgcn_make_buffer_rsrc((void*)p, (short)0, (int)bytes, 0x00020000);
}
template <int MC> __device__ __forceinline__ void dma64(unsigned mbase, unsigned voff, rsrc_t srd, unsigned soff) {
  asm volatile("s_add_u32 m0, %0, %4\n\ts_nop 0\n\tbuffer_load_dwordx4 %1, %2, %3 offen lds"
               :: "s"(mbase), "v"(voff), "s"(srd), "s"(soff), "i"(MC) : "memory", "scc");
}
template <int MC> __device__ __forceinline__ void dma64_o128(unsigned mbase, unsigned voff, rsrc_t srd, unsigned soff) {
  asm volatile("s_add_u32 m0, %0, %4\n\ts_nop 0\n\tbuffer_load_dwordx4 %1, %2, %3 offen offset:128 lds"
               :: "s"(mbase), "v"(voff), "s"(srd), "s"(soff), "i"(MC - 128) : "memory", "scc");
}
template <int MC> __device__ __forceinline__ void dma48(unsigned mbase, unsigned voff, rsrc_t srd, unsigned soff) {
  asm volatile("s_add_u32 m0, %0, %4\n\ts_mov_b32 exec_hi, 0xffff\n\tbuffer_load_dwordx4 %1, %2, %3 offen lds\n\ts_mov_b32 exec_hi, -1"
               :: "s"(mbase), "v"(voff), "s"(srd), "s"(soff), "i"(MC) : "memory", "scc");
}
template <int MC> __device__ __forceinline__ void dma32(unsigned mbase, unsigned voff, rsrc_t srd, unsigned soff) {
  asm volatile("s_add_u32 m0, %0, %4\n\ts_mov_b32 exec_hi, 0\n\tbuffer_load_dwordx4 %1, %2, %3 offen lds\n\ts_mov_b32 exec_hi, -1"
               :: "s"(mbase), "v"(voff), "s"(srd), "s"(soff), "i"(MC) : "memory", "scc");
}
__device__ __forceinline__ void touch(unsigned mscr, unsigned voff, rsrc_t srd) {
  asm volatile("s_mov_b32 m0, %0\n\ts_nop 0\n\tbuffer_load_dword %1, %2, 0 offen lds" :: "s"(mscr), "v"(voff), "s"(srd) : "memory");
}
__device__ __forceinline__ bf16x8 lds_b128(LAS char* p) { return *(LAS bf16x8*)p; }
__device__ __forceinline__ bf16x8 lds_tr2(LAS char* p) {
  s16x4 a = __builtin_amdgcn_ds_read_tr16_b64_v4i16((LAS s16x4*)p);
  s16x4 b = __builtin_amdgcn_ds_read_tr16_b64_v4i16((LAS s16x4*)(p + 128));
  s16x8 r = __builtin_shufflevector(a, b, 0, 1, 2, 3, 4, 5, 6, 7);
  return __builtin_bit_cast(bf16x8, r);
}
__device__ __forceinline__ unsigned pk_bf16(float lo, float hi) {
  bf16x2 v; v.x = (__bf16)lo; v.y = (__bf16)hi; return __builtin_bit_cast(unsigned, v);
}
__device__ __forceinline__ float bf_lo(unsigned u) { return __uint_as_float(u << 16); }
__device__ __forceinline__ float bf_hi(unsigned u) { return __uint_as_float(u & 0xffff0000u); }
#define MFMA16(a, b, c) __builtin_amdgcn_mfma_f32_16x16x32_bf16((a), (b), (c), 0, 0, 0)
#define MFMA32(a, b, c) __builtin_amdgcn_mfma_f32_32x32x16_bf16((a), (b), (c), 0, 0, 0)

__device__ __forceinline__ unsigned geglu2(float g0, float g1, float u0, float u1) {
  const float c1 = -2.0f * 0.7978845608028654f * 1.4426950408889634f;
  const float c2 = c1 * 0.044715f;
  const f32x2 g = {g0, g1}, u = {u0, u1}, k1 = {c1, c1}, k2 = {c2, c2}, one = {1.0f, 1.0f};
  const f32x2 z = g * __builtin_elementwise_fma(g * g, k2, k1);
  f32x2 e; e.x = __builtin_amdgcn_exp2f(z.x); e.y = __builtin_amdgcn_exp2f(z.y);
  const f32x2 d = e + one;
  f32x2 r; r.x = __builtin_amdgcn_rcpf(d.x); r.y = __builtin_amdgcn_rcpf(d.y);
  const f32x2 h = (g * u) * r;
  return pk_bf16(h.x, h.y);
}
__device__ __forceinline__ bool arrive(LAS unsigned* cnt, int lane, unsigned n) {
  unsigned old = 0;
  if (lane == 0) old = __hip_atomic_fetch_add(cnt, 1u, __ATOMIC_RELAXED, __HIP_MEMORY_SCOPE_WORKGROUP);
  return __builtin_amdgcn_readfirstlane(old) == n - 1u;
}

namespace k1 {
constexpr int XP = 1088, XH = 16 * XP;
constexpr int X0 = 0;
constexpr int GU_RB = 14 * 256 + 128, GU_ST = 8 * GU_RB, GU0 = 4 * XH;
constexpr int QK_RB = 6 * 256 + 128, QK_ST = 8 * QK_RB, QK0 = GU0 + 2 * GU_ST;
constexpr int SCR = QK0 + 2 * QK_ST, LDS_BYTES = SCR + 256;
constexpr unsigned OOB = 0x40000000u;

struct Ctx {
  LAS char* xr; LAS char* gr; LAS char* qr;
  unsigned mX, mG, mQ, mS, dB;
  unsigned vX, vG, vQ;
  rsrc_t sX, sG, sU, sQ, sV;
};

template <int ST> __device__ __forceinline__ void stage_Wa(const Ctx& c, unsigned kG) {
  dma64<GU0 + ST * GU_ST>(c.mG, c.vG, c.sG, kG);
  dma64<GU0 + ST * GU_ST + 1024>(c.mG, c.vG, c.sU, kG);
}
template <int ST> __device__ __forceinline__ void stage_Wb(const Ctx& c, unsigned kG, unsigned kQ) {
  dma48<GU0 + ST * GU_ST + 2048>(c.mG, c.vG, c.sG, kG + c.dB);
  dma48<GU0 + ST * GU_ST + 2816>(c.mG, c.vG, c.sU, kG + c.dB);
  dma64<QK0 + ST * QK_ST>(c.mQ, c.vQ, c.sQ, kQ);
  dma32<QK0 + ST * QK_ST + 1024>(c.mQ, c.vQ, c.sV, kQ);
}
template <int ST, int AI> __device__ __forceinline__ void stage_X(const Ctx& c, unsigned kX) {
  dma64<X0 + (ST * 2 + AI) * XH>(c.mX, c.vX, c.sX, kX + AI * 262144u);
  dma64<X0 + (ST * 2 + AI) * XH + 8 * XP>(c.mX, c.vX, c.sX, kX + AI * 262144u + 131072u);
}
template <int ST, int AI> __device__ __forceinline__ void read_X(const Ctx& c, bf16x8 (&xf)[4][2]) {
#pragma unroll
  for (int mt = 0; mt < 4; ++mt)
#pragma unroll
    for (int kk = 0; kk < 2; ++kk) xf[mt][kk] = lds_b128(c.xr + (ST * 2 + AI) * XH + mt * 2 * XP + kk * 64);
}
template <int ST> __device__ __forceinline__ void read_Wa(const Ctx& c, bf16x8 (&wa)[2][2]) {
#pragma unroll
  for (int kk = 0; kk < 2; ++kk) {
    wa[0][kk] = lds_tr2(c.gr + ST * GU_ST + kk * 4 * GU_RB);
    wa[1][kk] = lds_tr2(c.gr + ST * GU_ST + kk * 4 * GU_RB + 1024);
  }
}
template <int ST, bool W3> __device__ __forceinline__ void read_Wb(const Ctx& c, bf16x8 (&wb)[3][2]) {
#pragma unroll
  for (int kk = 0; kk < 2; ++kk) {
    if constexpr (!W3) {
      wb[0][kk] = lds_tr2(c.gr + ST * GU_ST + kk * 4 * GU_RB + 2048);
      wb[1][kk] = lds_tr2(c.gr + ST * GU_ST + kk * 4 * GU_RB + 2816);
      wb[2][kk] = lds_tr2(c.qr + ST * QK_ST + kk * 4 * QK_RB);
    } else {
      wb[0][kk] = lds_tr2(c.qr + ST * QK_ST + kk * 4 * QK_RB);
      wb[1][kk] = lds_tr2(c.qr + ST * QK_ST + kk * 4 * QK_RB + 256);
      wb[2][kk] = lds_tr2(c.qr + ST * QK_ST + kk * 4 * QK_RB + 512);
    }
  }
}
#define PIN_X(xf) asm volatile("" : "+v"(xf[0][0]), "+v"(xf[0][1]), "+v"(xf[1][0]), "+v"(xf[1][1]), "+v"(xf[2][0]), "+v"(xf[2][1]), "+v"(xf[3][0]), "+v"(xf[3][1]))
__device__ __forceinline__ void mma_a(f32x4 (&acc)[4][5], bf16x8 (&xf)[4][2], bf16x8 (&wa)[2][2]) {
  PIN_X(xf);
  __builtin_amdgcn_s_setprio(1);
#pragma unroll
  for (int s = 0; s < 2; ++s)
#pragma unroll
    for (int mt = 0; mt < 4; ++mt) {
#pragma unroll
      for (int kk = 0; kk < 2; ++kk) acc[mt][s] = MFMA16(wa[s][kk], xf[mt][kk], acc[mt][s]);
      asm volatile("" : "+v"(acc[mt][s])); SCHED();
    }
  __builtin_amdgcn_s_setprio(0);
  asm volatile("" : "+v"(acc[0][0]), "+v"(acc[0][1]), "+v"(acc[1][0]), "+v"(acc[1][1]), "+v"(acc[2][0]), "+v"(acc[2][1]), "+v"(acc[3][0]), "+v"(acc[3][1]));
}
template <bool W3> __device__ __forceinline__ void mma_b(f32x4 (&acc)[4][5], bf16x8 (&xf)[4][2], bf16x8 (&wb)[3][2]) {
  PIN_X(xf);
  __builtin_amdgcn_s_setprio(1);
#pragma unroll
  for (int s = 0; s < 3; ++s)
#pragma unroll
    for (int mt = 0; mt < 4; ++mt) {
#pragma unroll
      for (int kk = 0; kk < 2; ++kk) {
        if (W3 && s > 0) acc[mt][2 + s] = MFMA16(xf[mt][kk], wb[s][kk], acc[mt][2 + s]);
        else             acc[mt][2 + s] = MFMA16(wb[s][kk], xf[mt][kk], acc[mt][2 + s]);
      }
      asm volatile("" : "+v"(acc[mt][2 + s])); SCHED();
    }
  __builtin_amdgcn_s_setprio(0);
  asm volatile("" : "+v"(acc[0][2]), "+v"(acc[0][3]), "+v"(acc[0][4]), "+v"(acc[1][2]), "+v"(acc[1][3]), "+v"(acc[1][4]),
                    "+v"(acc[2][2]), "+v"(acc[2][3]), "+v"(acc[2][4]), "+v"(acc[3][2]), "+v"(acc[3][3]), "+v"(acc[3][4]));
}

template <int ST, bool W3>
__device__ __forceinline__ void ktile(const Ctx& c, f32x4 (&acc)[2][4][5], bf16x8 (&xf)[4][2], bf16x8 (&wa)[2][2], bf16x8 (&wb)[3][2],
                                      unsigned kX, unsigned kG, unsigned kQ) {
  read_X<ST, 0>(c, xf); stage_Wa<ST>(c, kG);
  WAIT_VL(14); SCHED(); BAR(); SCHED();
  mma_a(acc[0], xf, wa);
  SCHED(); BAR(); SCHED();
  read_Wb<ST, W3>(c, wb); stage_X<ST, 0>(c, kX);
  WAIT_VL(14); SCHED(); BAR(); SCHED();
  mma_b<W3>(acc[0], xf, wb);
  SCHED(); BAR(); SCHED();
  read_X<ST, 1>(c, xf); stage_Wb<ST>(c, kG, kQ);
  WAIT_VL(16); SCHED(); BAR(); SCHED();
  mma_a(acc[1], xf, wa);
  SCHED(); BAR(); SCHED();
  read_Wa<ST ^ 1>(c, wa); stage_X<ST, 1>(c, kX);
  WAIT_VL(16); SCHED(); BAR(); SCHED();
  mma_b<W3>(acc[1], xf, wb);
  SCHED(); BAR(); SCHED();
}


constexpr int A_K = 66560, A_V = A_K + 32768, A_Q = A_V + 32768, A_CNT = A_Q + 16384;
template <bool W3>
__device__ __forceinline__ void body(const Ctx& c, LAS char* lds, int wid, int pm, int j, char* ws, const u16* wd, const u16* wo, u64& tag, const u16* x, const u16* gw, int rowq, int row3, u64& gra, u64& grb) {
  const int wr = wid >> 2, wc = wid & 3;
  f32x4 acc[2][4][5];
#pragma unroll
  for (int a = 0; a < 2; ++a)
#pragma unroll
    for (int m = 0; m < 4; ++m)
#pragma unroll
      for (int s = 0; s < 5; ++s) acc[a][m][s] = (f32x4){0.f, 0.f, 0.f, 0.f};
  bf16x8 xf[4][2], wa[2][2], wb[3][2];
  const int grow = pm * 256 + j * 8 + wid;
  u32x4 gx0, gx1, gw0, gw1; unsigned epv;
  {
    asm volatile("global_load_dword %0, %1, off sc1" : "=v"(epv) : "v"(ws + WS_FLAG) : "memory");
    const unsigned ln_ = __builtin_amdgcn_mbcnt_hi(~0u, __builtin_amdgcn_mbcnt_lo(~0u, 0u));
    const char* xp = (const char*)(x + (size_t)grow * D_) + ln_ * 32; const char* gp = (const char*)gw + ln_ * 32;
    asm volatile("global_load_dwordx4 %0, %4, off\n\tglobal_load_dwordx4 %1, %4, off offset:16\n\tglobal_load_dwordx4 %2, %5, off\n\tglobal_load_dwordx4 %3, %5, off offset:16"
                 : "=&v"(gx0), "=&v"(gx1), "=&v"(gw0), "=&v"(gw1) : "v"(xp), "v"(gp) : "memory");
  }
  const unsigned rot = (unsigned)((pm + (j >> 2)) & 1) * 8u;
  {
    const unsigned r0X = rot * 128u, r0G = rot * 64u * 7168u, r0Q = rot * 64u * 6144u;
    stage_Wa<0>(c, r0G); stage_X<0, 0>(c, r0X); stage_Wb<0>(c, r0G, r0Q); stage_X<0, 1>(c, r0X);
    stage_Wa<1>(c, r0G + 64u * 7168u); stage_X<1, 0>(c, r0X + 128); stage_Wb<1>(c, r0G + 64u * 7168u, r0Q + 64u * 6144u); stage_X<1, 1>(c, r0X + 128);
  }
  asm volatile("s_waitcnt vmcnt(20)" : "+v"(gx0), "+v"(gx1), "+v"(gw0), "+v"(gw1), "+v"(epv) :: "memory");
  {
    unsigned ep = __builtin_amdgcn_readfirstlane(epv) + 1u;
    asm volatile("" : "+s"(ep));
    tag = ((u64)ep << 32) | (u64)(ep ^ 0x9E3779B9u);
    float s = 0.f;
#pragma unroll
    for (int e = 0; e < 4; ++e) s += bf_lo(gx0[e]) * bf_lo(gw0[e]) + bf_hi(gx0[e]) * bf_hi(gw0[e]) + bf_lo(gx1[e]) * bf_lo(gw1[e]) + bf_hi(gx1[e]) * bf_hi(gw1[e]);
#pragma unroll
    for (int o = 32; o > 0; o >>= 1) s += __shfl_xor(s, o);
    const float g = 1.0f / (1.0f + __expf(-s));
    if (__builtin_amdgcn_mbcnt_hi(~0u, __builtin_amdgcn_mbcnt_lo(~0u, 0u)) == 0)
      __hip_atomic_store((u64*)(ws + WS_G) + grow, (tag & 0xffffffff00000000ull) | (u64)__float_as_uint(g), __ATOMIC_RELAXED, __HIP_MEMORY_SCOPE_AGENT);
  }
  SCHED();
  WAIT_V(17); BAR();
  if (wr == 1) BAR();
  read_Wa<0>(c, wa);
  WAIT_L0(); SCHED(); BAR(); SCHED(); BAR(); SCHED();
#pragma unroll 1
  for (int t = 0; t < 16; t += 2) {
    if (t == 14) {
      const unsigned ln_ = __builtin_amdgcn_mbcnt_hi(~0u, __builtin_amdgcn_mbcnt_lo(~0u, 0u));
      const unsigned gl = (ln_ * 2048u + blockIdx.x * 8u + (unsigned)wid) * 128u;
      touch(c.mS, gl, mk_rsrc(wd, F_ * D_ * 2)); touch(c.mS, gl, mk_rsrc(wo, D_ * D_ * 2));
    }
    const bool more = t < 14;
    const unsigned tn = ((unsigned)(t + 2) + rot) & 15u;
    unsigned kX = more ? tn * 128u : OOB;
    unsigned kG = more ? tn * 64u * 7168u : OOB;
    unsigned kQ = more ? tn * 64u * 6144u : OOB;
    ktile<0, W3>(c, acc, xf, wa, wb, kX, kG, kQ);
    ktile<1, W3>(c, acc, xf, wa, wb, kX + 128u, kG + 64u * 7168u, kQ + 64u * 6144u);
  }
  int lane = __builtin_amdgcn_mbcnt_hi(~0u, __builtin_amdgcn_mbcnt_lo(~0u, 0u)); asm volatile("" : "+v"(lane));
  const int fr = lane & 15, fq = lane >> 4;
  u32x4 qk[4], vv[8];
  auto pack16 = [&](const f32x4& x0, const f32x4& x1) {
    auto r0 = __builtin_amdgcn_permlane16_swap(pk_bf16(x0[0], x0[1]), pk_bf16(x1[0], x1[1]), false, false);
    auto r1 = __builtin_amdgcn_permlane16_swap(pk_bf16(x0[2], x0[3]), pk_bf16(x1[2], x1[3]), false, false);
    u32x4 w; w.x = r0[0]; w.y = r1[0]; w.z = r0[1]; w.w = r1[1]; return w;
  };
  auto pack32 = [&](const f32x4& x0, const f32x4& x1) {
    auto r0 = __builtin_amdgcn_permlane32_swap(pk_bf16(x0[0], x0[1]), pk_bf16(x1[0], x1[1]), false, false);
    auto r1 = __builtin_amdgcn_permlane32_swap(pk_bf16(x0[2], x0[3]), pk_bf16(x1[2], x1[3]), false, false);
    u32x4 w; w.x = r0[0]; w.y = r1[0]; w.z = r0[1]; w.w = r1[1]; return w;
  };
  const int qo = wr * 4096 + (wc & 1) * 1024 + (fq >> 1) * 512 + ((fq & 1) * 16 + fr) * 16;
  const int vo = wr * 4096 + (fq & 1) * 512 + ((fq >> 1) * 16 + fr) * 16;
#pragma unroll
  for (int i = 0; i < 4; ++i) qk[i] = pack16(acc[i >> 1][2 * (i & 1)][W3 ? 2 : 4], acc[i >> 1][2 * (i & 1) + 1][W3 ? 2 : 4]);
  if constexpr (W3) {
#pragma unroll
    for (int i = 0; i < 8; ++i) vv[i] = pack32(acc[i >> 2][i & 3][3], acc[i >> 2][i & 3][4]);
  }
  if (wc >= 2) {
    const rsrc_t sKV = mk_rsrc(ws + WS_KV + (size_t)(pm * 32 + j) * 32768, 32768);
#pragma unroll
    for (int i = 0; i < 4; ++i) __builtin_amdgcn_raw_buffer_store_b128(qk[i], sKV, (unsigned)(qo + ((i >> 1) * 4 + (i & 1)) * 2048), 0, 16);
    if constexpr (W3) {
#pragma unroll
      for (int i = 0; i < 8; ++i) __builtin_amdgcn_raw_buffer_store_b128(vv[i], sKV, (unsigned)(16384 + vo + ((i >> 2) * 8 + (i & 3)) * 1024), 0, 16);
    }
  }
  SCHED();
  asm volatile("global_load_dwordx2 %0, %2, off sc1\n\tglobal_load_dwordx2 %1, %3, off sc1" : "=&v"(gra), "=&v"(grb)
               : "v"((const u64*)(ws + WS_G) + rowq + (lane & 31)), "v"((const u64*)(ws + WS_G) + row3 + lane) : "memory");
  if (wr == 0) BAR();
  if constexpr (W3) WAIT_V(14); else { if (wc == 2) WAIT_V(6); else WAIT_V(2); }
  BAR();
  {
    LAS char* p = lds + (wc >= 2 ? A_K + 16384 : A_Q) + qo;
#pragma unroll
    for (int i = 0; i < 4; ++i) *(LAS u32x4*)(p + ((i >> 1) * 4 + (i & 1)) * 2048) = qk[i];
    if constexpr (W3) {
#pragma unroll
      for (int i = 0; i < 8; ++i) *(LAS u32x4*)(lds + A_V + 16384 + vo + ((i >> 2) * 8 + (i & 3)) * 1024) = vv[i];
    }
  }
  if (wid == 0 && lane < 4) ((LAS unsigned*)(lds + A_CNT))[lane] = 0u;
  WAIT_L0(); BAR();
  auto kv_done = [&]() {
    if (arrive((LAS unsigned*)(lds + A_CNT), lane, 4u) && lane == 0)
      __hip_atomic_store((u64*)(ws + WS_FLAG + FL_KV) + pm * 32 + j, tag, __ATOMIC_RELAXED, __HIP_MEMORY_SCOPE_AGENT);
  };
  if constexpr (W3) { WAIT_V(0); kv_done(); }
  SCHED();
  const rsrc_t sA = mk_rsrc(ws + WS_A2, A2_BYTES);
  const unsigned hoff = (unsigned)((((j >> 2) * 28 + (j & 3) * 4 + wc) * 2048 + pm * 256 + wr * 64 + (fq & 1) * 16 + fr) * 32 + (fq >> 1) * 16);
  auto hstore = [&](int a, int mp, int sg, unsigned so) {
    const f32x4 g0 = acc[a][2 * mp][sg], u0 = acc[a][2 * mp][sg + 1], g1 = acc[a][2 * mp + 1][sg], u1 = acc[a][2 * mp + 1][sg + 1];
    const unsigned a0 = geglu2(g0[0], g0[1], u0[0], u0[1]), a1 = geglu2(g0[2], g0[3], u0[2], u0[3]);
    const unsigned b0 = geglu2(g1[0], g1[1], u1[0], u1[1]), b1 = geglu2(g1[2], g1[3], u1[2], u1[3]);
    auto r0 = __builtin_amdgcn_permlane16_swap(a0, b0, false, false);
    auto r1 = __builtin_amdgcn_permlane16_swap(a1, b1, false, false);
    u32x4 w; w.x = r0[0]; w.y = r1[0]; w.z = r0[1]; w.w = r1[1];
    __builtin_amdgcn_raw_buffer_store_b128(w, sA, hoff + so + (unsigned)((a * 128 + mp * 32) * 32), 0, 16);
  };
  hstore(0, 0, 0, 0);
  SCHED();
  if constexpr (!W3) { if (wc == 2) { WAIT_V(1); kv_done(); } }
  SCHED();
  hstore(0, 1, 0, 0); hstore(1, 0, 0, 0); hstore(1, 1, 0, 0);
  SCHED();
  asm volatile("s_waitcnt vmcnt(4)" : "+v"(gra), "+v"(grb) :: "memory");
  SCHED();
  if constexpr (!W3) {
    const unsigned d2 = (unsigned)(16 - (j & 3)) * 65536u;
    hstore(0, 0, 2, d2); hstore(0, 1, 2, d2); hstore(1, 0, 2, d2); hstore(1, 1, 2, d2);
  }
}
}

namespace k2 {
constexpr int B_RB = 1024 + 32, B_H = 8 * B_RB, ST_A = 16384, ST_B = 2 * B_H, ST = ST_A + ST_B;
__device__ __forceinline__ bf16x8 lds_tr2b(LAS char* p) {
  s16x4 a = __builtin_amdgcn_ds_read_tr16_b64_v4i16((LAS s16x4*)p);
  s16x4 b = __builtin_amdgcn_ds_read_tr16_b64_v4i16((LAS s16x4*)(p + 512));
  s16x8 r = __builtin_shufflevector(a, b, 0, 1, 2, 3, 4, 5, 6, 7);
  return __builtin_bit_cast(bf16x8, r);
}
__device__ __forceinline__ void wait_tags(const u64* fl, int n, u64 tag, int lane) {
  const u64* p = fl + (lane < n ? lane : 0);
  for (unsigned spins = 0; spins < (1u << 18); ++spins) {
    if (__all(__hip_atomic_load(p, __ATOMIC_RELAXED, __HIP_MEMORY_SCOPE_AGENT) == tag)) break;
    __builtin_amdgcn_s_sleep(1);
  }
}
__device__ __forceinline__ float read_gate(const char* ws, int row, u64 tag) {
  const u64* p = (const u64*)(ws + WS_G) + row;
  u64 v = 0;
  for (unsigned spins = 0; spins < (1u << 18); ++spins) {
    v = __hip_atomic_load(p, __ATOMIC_RELAXED, __HIP_MEMORY_SCOPE_AGENT);
    if (__all((v >> 32) == (tag >> 32))) break;
    __builtin_amdgcn_s_sleep(1);
  }
  return __uint_as_float((unsigned)v);
}
template <int I0, int NT, bool FIRST>
__device__ __forceinline__ void attn_chunk(LAS char* kl, LAS char* vl, bf16x8 qf0, bf16x8 qf1, int i0, int l31, int hh, float& mrun, f32x2& lrun, f32x16& oacc) {
  const float NEG = -__builtin_inff();
  const float cs = 0.17677669529663687f * 1.4426950408889634f;
  f32x16 sc[NT];
#pragma unroll
  for (int i = 0; i < NT; ++i) {
    f32x16 z = {};
    z = MFMA32(lds_b128(kl + (I0 + i) * 2048), qf0, z);
    sc[i] = MFMA32(lds_b128(kl + (I0 + i) * 2048 + 1024), qf1, z);
  }
#pragma unroll
  for (int r = 0; r < 16; ++r) {
    const int kl_ = (r & 3) + 8 * (r >> 2) + 4 * hh;
    if constexpr (I0 == 0) sc[0][r] = (kl_ > l31) ? sc[0][r] : NEG;
    if constexpr (I0 + NT == 9) sc[NT - 1][r] = (kl_ <= l31) ? sc[NT - 1][r] : NEG;
  }
  float mx = mrun;
#pragma unroll
  for (int i = 0; i < NT; ++i) {
    float ma = fmaxf(sc[i][0], sc[i][1]), mb = fmaxf(sc[i][2], sc[i][3]);
#pragma unroll
    for (int r = 4; r < 16; r += 4) { ma = fmaxf(fmaxf(ma, sc[i][r]), sc[i][r + 1]); mb = fmaxf(fmaxf(mb, sc[i][r + 2]), sc[i][r + 3]); }
    mx = fmaxf(mx, fmaxf(ma, mb) + ((I0 + i < i0) ? NEG : 0.f));
  }
  mx = fmaxf(mx, __shfl_xor(mx, 32));
  if constexpr (!FIRST) {
    const float al = __builtin_amdgcn_exp2f((mrun - mx) * cs);
    lrun *= al;
#pragma unroll
    for (int r = 0; r < 16; ++r) oacc[r] *= al;
  }
  mrun = mx;
  const f32x2 cs2 = {cs, cs};
  const float mc = -mx * cs;
  f32x2 l2[2] = {lrun, {0.f, 0.f}};
#pragma unroll
  for (int i = 0; i < NT; ++i) {
    const float mci = mc + ((I0 + i < i0) ? NEG : 0.f);
    const f32x2 mc2 = {mci, mci};
#pragma unroll
    for (int s = 0; s < 2; ++s) {
      u32x4 pw;
#pragma unroll
      for (int k = 0; k < 4; ++k) {
        const f32x2 sv = {sc[i][8 * s + 2 * k], sc[i][8 * s + 2 * k + 1]};
        const f32x2 t = __builtin_elementwise_fma(sv, cs2, mc2);
        f32x2 p; p.x = __builtin_amdgcn_exp2f(t.x); p.y = __builtin_amdgcn_exp2f(t.y);
        l2[k & 1] += p;
        pw[k] = pk_bf16(p.x, p.y);
      }
      oacc = MFMA32(lds_b128(vl + (I0 + i) * 2048 + s * 1024), __builtin_bit_cast(bf16x8, pw), oacc);
    }
  }
  lrun = l2[0] + l2[1];
}
__device__ __forceinline__ void lds_wait(LAS unsigned* w, unsigned n) {
  for (unsigned spins = 0; spins < (1u << 22); ++spins) {
    if (__builtin_amdgcn_readfirstlane(__hip_atomic_load(w, __ATOMIC_RELAXED, __HIP_MEMORY_SCOPE_WORKGROUP)) >= n) break;
    __builtin_amdgcn_s_sleep(1);
  }
  asm volatile("" ::: "memory");
}
__device__ __forceinline__ void attn_finish(const f32x16& oacc, f32x2 lrun, int row0, int lane, rsrc_t sA, int head, float gq) {
  const int l31 = lane & 31, hh = lane >> 5;
  float sum = lrun.x + lrun.y;
  sum += __shfl_xor(sum, 32);
  const float inv = gq / sum;
#pragma unroll
  for (int g2 = 0; g2 < 2; ++g2) {
    unsigned a0 = pk_bf16(oacc[8 * g2 + 0] * inv, oacc[8 * g2 + 1] * inv), a1 = pk_bf16(oacc[8 * g2 + 2] * inv, oacc[8 * g2 + 3] * inv);
    unsigned b0 = pk_bf16(oacc[8 * g2 + 4] * inv, oacc[8 * g2 + 5] * inv), b1 = pk_bf16(oacc[8 * g2 + 6] * inv, oacc[8 * g2 + 7] * inv);
    auto r0 = __builtin_amdgcn_permlane32_swap(a0, b0, false, false);
    auto r1 = __builtin_amdgcn_permlane32_swap(a1, b1, false, false);
    u32x4 w; w.x = r0[0]; w.y = r1[0]; w.z = r0[1]; w.w = r1[1];
    __builtin_amdgcn_raw_buffer_store_b128(w, sA, (unsigned)(((224 + head * 2 + g2) * 2048 + row0 + l31) * 32 + hh * 16), 0, 16);
  }
}

template <int MC> __device__ __forceinline__ void dma4k(unsigned mbase, unsigned voff, rsrc_t srd, unsigned soff) {
  asm volatile("s_add_u32 m0, %0, %4\n\ts_nop 0\n\tbuffer_load_dwordx4 %1, %2, %3 offen sc1 lds\n\tbuffer_load_dwordx4 %1, %2, %3 offen offset:1024 sc1 lds\n\t"
               "buffer_load_dwordx4 %1, %2, %3 offen offset:2048 sc1 lds\n\tbuffer_load_dwordx4 %1, %2, %3 offen offset:3072 sc1 lds"
               :: "s"(mbase), "v"(voff), "s"(srd), "s"(soff), "i"(MC) : "memory", "scc");
}

struct LCtx { unsigned vA, vB0, vB1, mA, mB; rsrc_t sA; };
template <int SL> __device__ __forceinline__ void stage(const LCtx& c, rsrc_t sB, unsigned ga, unsigned kb) {
  dma4k<SL * ST>(c.mA, c.vA, c.sA, ga);
  dma64<SL * ST>(c.mB, c.vB0, sB, kb); dma64_o128<SL * ST + B_H>(c.mB, c.vB0, sB, kb);
  dma64<SL * ST + B_RB>(c.mB, c.vB1, sB, kb); dma64_o128<SL * ST + B_RB + B_H>(c.mB, c.vB1, sB, kb);
}
struct Ctx { LAS char* ao; LAS char* ak; LAS char* br0; LAS char* br1; LAS char* ao2; LAS char* ak2; LAS char* br02; LAS char* br12; };
struct Frag { bf16x8 a[4][2], b[4][2]; };
template <int SL, int I> __device__ __forceinline__ void rd(const Ctx& c, Frag& f) {
  constexpr int so = (SL & 1) * ST, kk = I & 1, x = (I >> 1) & 3;
  LAS char* const ar = (x & 2) ? ((SL & 2) ? c.ak2 : c.ak) : ((SL & 2) ? c.ao2 : c.ao);
  LAS char* const br0 = (SL & 2) ? c.br02 : c.br0; LAS char* const br1 = (SL & 2) ? c.br12 : c.br1;
  if constexpr (I < 8) f.b[x][kk] = lds_tr2b(((x & 2) ? br1 : br0) + so + kk * 4 * B_RB + (x & 1) * 32);
  else                 f.a[x][kk] = lds_b128(ar + so + kk * 8192 + (x & 1) * 1024);
}
template <int SL, int P>
__device__ __forceinline__ void pairs(const Ctx& c, f32x4 (&acc)[4][4], Frag& f, Frag& n) {
  constexpr int ns = (SL + 1) & 3;
#pragma unroll
  for (int e = 0; e < 2; ++e) {
    const int m = (P + e) >> 2, nn = (P + e) & 3;
    acc[m][nn] = MFMA16(f.b[nn][0], f.a[m][0], acc[m][nn]);
    acc[m][nn] = MFMA16(f.b[nn][1], f.a[m][1], acc[m][nn]);
  }
  asm volatile("" : "+v"(acc[P >> 2][P & 3]), "+v"(acc[(P + 1) >> 2][(P + 1) & 3]));
  SCHED();
  if constexpr (P < 12) {
    constexpr int i0 = (P / 2) * 3;
    rd<ns, i0>(c, n);
    if constexpr (i0 + 1 < 16) rd<ns, i0 + 1>(c, n);
    if constexpr (i0 + 2 < 16) rd<ns, i0 + 2>(c, n);
  }
  SCHED();
}
template <int SL>
__device__ __forceinline__ void step(const Ctx& c, f32x4 (&acc)[4][4], Frag& f, Frag& n) {
  asm volatile("" : "+v"(f.a[0][0]), "+v"(f.a[0][1]), "+v"(f.a[1][0]), "+v"(f.a[1][1]), "+v"(f.a[2][0]), "+v"(f.a[2][1]), "+v"(f.a[3][0]), "+v"(f.a[3][1]));
  pairs<SL, 0>(c, acc, f, n); pairs<SL, 2>(c, acc, f, n); pairs<SL, 4>(c, acc, f, n); pairs<SL, 6>(c, acc, f, n);
  pairs<SL, 8>(c, acc, f, n); pairs<SL, 10>(c, acc, f, n); pairs<SL, 12>(c, acc, f, n); pairs<SL, 14>(c, acc, f, n);
  WAIT_L0(); SCHED(); BAR(); SCHED();
}
template <int H, int NS, int NH, bool RD>
__device__ __forceinline__ void half(const Ctx& c, f32x4 (&acc)[4][4], Frag& f, Frag& n) {
  asm volatile("" : "+v"(f.a[2 * H][0]), "+v"(f.a[2 * H][1]), "+v"(f.a[2 * H + 1][0]), "+v"(f.a[2 * H + 1][1]));
#define HP(P, R0, R1, R2, R3) do { \
    _Pragma("unroll") for (int e = 0; e < 2; ++e) { const int m = 2 * H + (((P) + e) >> 2), nn = ((P) + e) & 3; \
      acc[m][nn] = MFMA16(f.b[nn][0], f.a[m][0], acc[m][nn]); acc[m][nn] = MFMA16(f.b[nn][1], f.a[m][1], acc[m][nn]); } \
    asm volatile("" : "+v"(acc[2 * H + ((P) >> 2)][(P) & 3]), "+v"(acc[2 * H + (((P) + 1) >> 2)][((P) + 1) & 3])); SCHED(); \
    if constexpr (RD && (R0) >= 0) { rd<NS, (R0) < 0 ? 0 : (R0)>(c, n); rd<NS, (R1) < 0 ? 0 : (R1)>(c, n); rd<NS, (R2) < 0 ? 0 : (R2)>(c, n); rd<NS, (R3) < 0 ? 0 : (R3)>(c, n); } SCHED(); } while (0)
  HP(0, 0, 1, 2, 3); HP(2, 4, 5, 6, 7); HP(4, 8 + 4 * NH, 9 + 4 * NH, 10 + 4 * NH, 11 + 4 * NH); HP(6, -1, -1, -1, -1);
#undef HP
  WAIT_L0(); SCHED();
}
}

__global__ __launch_bounds__(512, 2) void fused_kernel(const u16* __restrict__ x, const u16* __restrict__ wqkv, const u16* __restrict__ wg, const u16* __restrict__ wu,
                                                       const u16* __restrict__ gw, const u16* __restrict__ wd, const u16* __restrict__ wo, char* __restrict__ ws,
                                                       u16* __restrict__ out) {
  extern __shared__ __attribute__((aligned(16))) char smem[];
  LAS char* lds = (LAS char*)smem;
  const unsigned lbase = (unsigned)(size_t)lds;
  const int wid = __builtin_amdgcn_readfirstlane(threadIdx.x >> 6);
  const int bid = blockIdx.x, xcd = bid & 7, idx = bid >> 3;
  const int j = xcd * 4 + (idx >> 3), pm = idx & 7;
  u64 tag, gra, grb;
  const int hb = xcd & 1;
  const int mt = (xcd >> 2) * 8 + (idx >> 2), nt = ((xcd >> 1) & 1) * 4 + (idx & 3), panel = mt >> 1;
  const int ql = (wid + 4) & 7;
  const int rowq = pm * 256 + ql * 32, row3 = mt * 128 + ((wid >> 1) & 1) * 64;
  {
    using namespace k1;
    const int lane = threadIdx.x & 63, wr = wid >> 2, wc = wid & 3;
    Ctx c;
    c.sX = mk_rsrc(x, S_ * D_ * 2);
    c.sG = mk_rsrc(wg, D_ * F_ * 2);
    c.sU = mk_rsrc(wu, D_ * F_ * 2);
    c.sQ = mk_rsrc(wqkv, D_ * 3072 * 2);
    c.sV = mk_rsrc((const char*)wqkv + 4096, D_ * 3072 * 2 - 4096);
    const int r = lane >> 3, cp = lane & 7, ch = (cp & 4) | ((cp & 3) ^ ((r >> 1) & 3));
    c.vX = (unsigned)((pm * 256 + wid * 8 + r) * 2048 + ch * 16);
    const int u = lane >> 4, rr = (lane >> 1) & 7, hf = lane & 1;
    c.vG = (unsigned)((wid * 8 + rr) * 7168 + (j >> 2) * 896 + (j & 3) * 128 + u * 32 + hf * 16);
    c.dB = (unsigned)(512 - 32 * (j & 3));
    c.vQ = (unsigned)((wid * 8 + rr) * 6144 + (u >> 1) * 2048 + j * 64 + (u & 1) * 32 + hf * 16);
    c.mS = lbase + SCR; c.mX = lbase + wid * XP; c.mG = lbase + wid * GU_RB; c.mQ = lbase + wid * QK_RB;
    const int fr = lane & 15, q = lane >> 4;
    c.xr = lds + X0 + (wr * 8 + (fr >> 3)) * XP + (fr & 7) * 128 + ((q ^ ((fr >> 1) & 3)) * 16);
    const int qq = (lane & 15) >> 2, p = lane & 3;
    c.gr = lds + GU0 + q * GU_RB + qq * 32 + p * 8 + wc * 256;
    c.qr = lds + QK0 + q * QK_RB + qq * 32 + p * 8 + wc * 256;
    if (wc == 3) body<true>(c, lds, wid, pm, j, ws, wd, wo, tag, x, gw, rowq, row3, gra, grb); else body<false>(c, lds, wid, pm, j, ws, wd, wo, tag, x, gw, rowq, row3, gra, grb);
  }
  using namespace k2;
  int lane = __builtin_amdgcn_mbcnt_hi(~0u, __builtin_amdgcn_mbcnt_lo(~0u, 0u)); asm volatile("" : "+v"(lane));
  const rsrc_t sA3 = mk_rsrc(ws + WS_A2, hb ? A2_BYTES : 36u * 262144u);
  const rsrc_t sD = mk_rsrc(wd, hb ? F_ * D_ * 2 : 36 * 131072), sO = mk_rsrc(wo, D_ * D_ * 2);
  const unsigned ga0 = hb ? 36u * 262144u : 0u, kb0 = hb ? 36u * 131072u : 0u;
  float g4;
  {
    const int l31 = lane & 31, hh = lane >> 5;
    LAS char* kl = lds + k1::A_K + ql * 2048 + lane * 16; LAS char* vl = lds + k1::A_V + ql * 2048 + lane * 16;
    const bf16x8 qf0 = lds_b128(lds + k1::A_Q + ql * 2048 + lane * 16), qf1 = lds_b128(lds + k1::A_Q + ql * 2048 + 1024 + lane * 16);
    float mrun = -__builtin_inff(); f32x2 lrun = {0.f, 0.f}; f32x16 oacc = {};
    auto h_done = [&]() {
      WAIT_V(0);
      if (arrive((LAS unsigned*)(lds + k1::A_CNT) + 1, lane, 8u) && lane == 0)
        __hip_atomic_store((u64*)(ws + WS_FLAG + FL_H) + pm * 32 + j, tag, __ATOMIC_RELAXED, __HIP_MEMORY_SCOPE_AGENT);
    };
    LAS unsigned* kvrdy = (LAS unsigned*)(lds + k1::A_CNT) + 2;
    if ((wid & 3) == 3) {
      const unsigned t = (unsigned)(wid >> 2), mb = lbase + t * ST;
      const unsigned vB = (unsigned)((lane >> 3) * 2048 + nt * 256 + (((lane & 7) ^ ((lane >> 2) & 4)) * 16));
#pragma unroll
      for (int rb = 0; rb < 8; ++rb) {
        dma64<ST_A>(mb + rb * B_RB, vB, sD, kb0 + t * 131072u + rb * 16384u);
        dma64_o128<ST_A + B_H>(mb + rb * B_RB, vB, sD, kb0 + t * 131072u + rb * 16384u);
      }
      h_done();
      if (pm > 0) { wait_tags((const u64*)(ws + WS_FLAG + FL_KV) + (pm - 1) * 32 + j, 1, tag, lane); asm volatile("" ::: "memory"); }
      const rsrc_t sKV = mk_rsrc(ws + WS_KV + (size_t)((pm > 0 ? pm - 1 : 0) * 32 + j) * 32768, pm > 0 ? 32768 : 0);
      const unsigned vl_ = (unsigned)(lane * 16), mk = lbase + (t ? k1::A_V : k1::A_K);
      dma4k<0>(mk, vl_, sKV, t * 16384u); dma4k<4096>(mk, vl_, sKV, t * 16384u + 4096u); dma4k<8192>(mk, vl_, sKV, t * 16384u + 8192u); dma4k<12288>(mk, vl_, sKV, t * 16384u + 12288u);
      WAIT_V(0);
      if (lane == 0) __hip_atomic_fetch_add(kvrdy, 1u, __ATOMIC_RELAXED, __HIP_MEMORY_SCOPE_WORKGROUP);
    }
    float gq = __uint_as_float((unsigned)gra); g4 = 1.0f - __uint_as_float((unsigned)grb);
    if (!__all((gra >> 32) == (tag >> 32) && (grb >> 32) == (tag >> 32))) {
      gq = read_gate(ws, rowq + l31, tag); g4 = 1.0f - read_gate(ws, row3 + lane, tag);
    }
    const int i0 = pm > 0 ? 0 : 8 - ql;
    if (wid >= 4) { if (wid != 7) h_done(); lds_wait(kvrdy, 2u); }
    attn_chunk<4, 5, true>(kl, vl, qf0, qf1, i0, l31, hh, mrun, lrun, oacc);
    asm volatile("" : "+v"(oacc), "+v"(lrun), "+v"(mrun)); SCHED();
    if (wid < 4) { if (wid != 3) h_done(); lds_wait(kvrdy, 2u); }
    attn_chunk<0, 4, false>(kl, vl, qf0, qf1, i0, l31, hh, mrun, lrun, oacc);
    attn_finish(oacc, lrun, rowq, lane, mk_rsrc(ws + WS_A2, A2_BYTES), j, gq);
    if (wid < 2) {
      wait_tags((const u64*)(ws + WS_FLAG + FL_H) + panel * 32, 32, tag, lane);
      asm volatile("" ::: "memory");
      const unsigned vA = (unsigned)(mt * 128 * 32 + lane * 16);
      const unsigned t = (unsigned)wid, mb = lbase + t * ST;
#pragma unroll
      for (int g = 0; g < 4; ++g) dma4k<0>(mb + g * 4096, vA, sA3, ga0 + t * 262144u + g * 65536u);
    }
  }
  WAIT_VL(0); BAR();
  if (wid == 0 && lane == 0) __hip_atomic_store((u64*)(ws + WS_FLAG + FL_O) + pm * 32 + j, tag, __ATOMIC_RELAXED, __HIP_MEMORY_SCOPE_AGENT);
  const int q_sw = hb ? 4 : 1000;
  if (wid >= 4) {
    const int l = wid - 4;
    LCtx c;
    c.sA = sA3;
    c.vA = (unsigned)((l * 2048 + mt * 128) * 32 + lane * 16);
    c.vB0 = (unsigned)((l * 16 + (lane >> 3)) * 2048 + nt * 256 + (((lane & 7) ^ ((lane >> 2) & 4)) * 16)); c.vB1 = c.vB0 + 8u * 2048u;
    c.mA = lbase + l * 4096; c.mB = lbase + ST_A + l * 2 * B_RB;
    rsrc_t sB = sD;
    unsigned ga = ga0 + 4u * 262144u, kb = kb0 + 4u * 131072u;
    stage<2>(c, sB, ga0 + 2u * 262144u, kb0 + 2u * 131072u); stage<3>(c, sB, ga0 + 3u * 262144u, kb0 + 3u * 131072u);
    BAR();
#pragma unroll 1
    for (int q = 0; q < 8; ++q) {
      if (q == q_sw - 1 && l == 0) { wait_tags((const u64*)(ws + WS_FLAG + FL_O) + panel * 32, 32, tag, lane); asm volatile("" ::: "memory"); }
      if (q == q_sw) { sB = sO; kb = 0; }
      stage<0>(c, sB, ga, kb); WAIT_V(16); BAR();
      stage<1>(c, sB, ga + 262144u, kb + 131072u); WAIT_V(16); BAR();
      stage<2>(c, sB, ga + 2u * 262144u, kb + 2u * 131072u); WAIT_V(16); BAR();
      stage<3>(c, sB, ga + 3u * 262144u, kb + 3u * 131072u); WAIT_V(16); BAR();
      ga += 4u * 262144u; kb += 4u * 131072u;
    }
    WAIT_V(8); BAR();
    WAIT_V(0); BAR();
    if (l == 0) { wait_tags((const u64*)(ws + WS_FLAG + FL_P) + (bid ^ 1) * 8, 1, tag, lane); asm volatile("" ::: "memory"); }
    BAR();
  } else {
    const int wm = wid >> 1, wn = wid & 1;
    const int fr = lane & 15, fq = lane >> 4;
    float og[4];
#pragma unroll
    for (int m = 0; m < 4; ++m) og[m] = __shfl(g4, (m & 1) * 32 + (m < 2 ? 1 - hb : hb) * 16 + fr);
    asm volatile("" : "+v"(og[0]), "+v"(og[1]), "+v"(og[2]), "+v"(og[3]));
    Ctx c;
    c.ao = lds + (fq >> 1) * 4096 + (wm * 64 + (1 - hb) * 16 + fr) * 32 + (fq & 1) * 16;
    c.ak = lds + (fq >> 1) * 4096 + (wm * 64 + hb * 16 + fr) * 32 + (fq & 1) * 16;
    c.br0 = lds + ST_A + wn * B_H + fq * B_RB + (fr >> 2) * 128 + ((fr >> 3) * 64) + (lane & 3) * 8;
    c.br1 = lds + ST_A + wn * B_H + fq * B_RB + (fr >> 2) * 128 + (((fr >> 3) ^ 1) * 64) + (lane & 3) * 8;
    c.ao2 = c.ao + 2 * ST; c.ak2 = c.ak + 2 * ST; c.br02 = c.br0 + 2 * ST; c.br12 = c.br1 + 2 * ST;
    asm volatile("" : "+v"(c.ao2), "+v"(c.ak2), "+v"(c.br02), "+v"(c.br12));
    f32x4 acc[4][4];
#pragma unroll
    for (int m = 0; m < 4; ++m)
#pragma unroll
      for (int n = 0; n < 4; ++n) acc[m][n] = (f32x4){0.f, 0.f, 0.f, 0.f};
    Frag f0, f1;
    rd<0, 0>(c, f0); rd<0, 1>(c, f0); rd<0, 2>(c, f0); rd<0, 3>(c, f0); rd<0, 4>(c, f0); rd<0, 5>(c, f0); rd<0, 6>(c, f0); rd<0, 7>(c, f0);
    rd<0, 8>(c, f0); rd<0, 9>(c, f0); rd<0, 10>(c, f0); rd<0, 11>(c, f0); rd<0, 12>(c, f0); rd<0, 13>(c, f0); rd<0, 14>(c, f0); rd<0, 15>(c, f0);
    WAIT_L0(); SCHED(); BAR(); SCHED();
#pragma unroll 1
    for (int q = 0; q < 8; ++q) {
      if (q == q_sw + 1) {
        asm volatile("" : "+v"(og[0]), "+v"(og[1]), "+v"(og[2]), "+v"(og[3]));
#pragma unroll
        for (int m = 0; m < 4; ++m)
#pragma unroll
          for (int n = 0; n < 4; ++n) acc[m][n] *= og[m];
      }
      step<0>(c, acc, f0, f1); step<1>(c, acc, f1, f0); step<2>(c, acc, f0, f1); step<3>(c, acc, f1, f0);
    }
    half<0, 1, 0, true>(c, acc, f0, f1); BAR(); SCHED();
    half<0, 2, 0, true>(c, acc, f1, f0); BAR(); SCHED();
    half<0, 3, 0, true>(c, acc, f0, f1);
    half<0, 0, 1, true>(c, acc, f1, f0);
    const rsrc_t sPo = mk_rsrc(ws + WS_PART + (size_t)bid * 32768, 32768), sPi = mk_rsrc(ws + WS_PART + (size_t)(bid ^ 1) * 32768, 32768);
    const float sc0 = hb ? 1.0f : og[0], sc1_ = hb ? 1.0f : og[1], sc2 = hb ? 1.0f : og[2], sc3 = hb ? 1.0f : og[3];
#pragma unroll
    for (int n = 0; n < 4; ++n) {
      __builtin_amdgcn_raw_buffer_store_b128(__builtin_bit_cast(u32x4, acc[0][n] * sc0), sPo, (unsigned)(wid * 8192 + lane * 16 + n * 1024), 0, 16);
      __builtin_amdgcn_raw_buffer_store_b128(__builtin_bit_cast(u32x4, acc[1][n] * sc1_), sPo, (unsigned)(wid * 8192 + lane * 16 + (4 + n) * 1024), 0, 16);
    }
    SCHED();
    half<1, 1, 1, true>(c, acc, f0, f1);
    half<1, 2, 1, true>(c, acc, f1, f0);
    half<1, 3, 1, true>(c, acc, f0, f1);
    half<1, 0, 0, false>(c, acc, f1, f0);
    f32x4 kp[2][4];
#pragma unroll
    for (int n = 0; n < 4; ++n) { kp[0][n] = acc[2][n] * sc2; kp[1][n] = acc[3][n] * sc3; }
    WAIT_V(0);
    if (arrive((LAS unsigned*)(lds + k1::A_CNT) + 3, lane, 4u) && lane == 0)
      __hip_atomic_store((u64*)(ws + WS_FLAG + FL_P) + bid * 8, tag, __ATOMIC_RELAXED, __HIP_MEMORY_SCOPE_AGENT);
    BAR();
    u16* op = out + (size_t)(mt * 128 + wm * 64 + hb * 16 + fr) * D_ + nt * 128 + wn * 64 + fq * 4;
    f32x4 pp[2][4];
#pragma unroll
    for (int m = 0; m < 2; ++m)
#pragma unroll
      for (int n = 0; n < 4; ++n)
        pp[m][n] = __builtin_bit_cast(f32x4, __builtin_amdgcn_raw_buffer_load_b128(sPi, (unsigned)(wid * 8192 + lane * 16 + (m * 4 + n) * 1024), 0, 16));
#pragma unroll
    for (int m = 0; m < 2; ++m)
#pragma unroll
      for (int n = 0; n < 4; ++n) {
        const f32x4 v = kp[m][n] + pp[m][n];
        u32x2 w; w.x = pk_bf16(v[0], v[1]); w.y = pk_bf16(v[2], v[3]);
        *(u32x2*)(op + m * 32 * D_ + n * 16) = w;
      }
  }
  if (bid == 0 && wid == 5) {
#pragma unroll 1
    for (int i = 0; i < 4; ++i) wait_tags((const u64*)(ws + WS_FLAG + FL_H) + i * 64, 64, tag, lane);
    if (lane == 0) __hip_atomic_store((unsigned*)(ws + WS_FLAG), (unsigned)(tag >> 32), __ATOMIC_RELAXED, __HIP_MEMORY_SCOPE_AGENT);
  }
}

extern "C" void kernel_launch(void* const* d_in, const int* in_sizes, int n_in, void* d_out, int out_size, void* d_ws, size_t ws_size,
                              hipStream_t stream) {
  const u16* x = (const u16*)d_in[0]; const u16* wqkv = (const u16*)d_in[1]; const u16* wo = (const u16*)d_in[2];
  const u16* wg = (const u16*)d_in[4]; const u16* wu = (const u16*)d_in[5]; const u16* wd = (const u16*)d_in[6]; const u16* gw = (const u16*)d_in[7];
  (void)hipFuncSetAttribute((const void*)fused_kernel, hipFuncAttributeMaxDynamicSharedMemorySize, k1::LDS_BYTES);
  fused_kernel<<<256, 512, k1::LDS_BYTES, stream>>>(x, wqkv, wg, wu, gw, wd, wo, (char*)d_ws, (u16*)d_out);
}
```
